```python
import torch

def _softmax(x, axis=-1):
    return torch.softmax(x, dim=axis)
def _gelu(x):
    return torch.nn.functional.gelu(x, approximate="tanh")
def _silu(x):
    return torch.nn.functional.silu(x)
def _sigmoid(x):
    return torch.sigmoid(x)
def _layernorm(x, axis=-1, eps=1e-5):
    m = x.mean(dim=axis, keepdim=True)
    v = x.var(dim=axis, keepdim=True, unbiased=False)
    return (x - m) / torch.sqrt(v + eps)
def _scatter_add(tgt, idx, src, axis=0):
    return tgt.clone().index_add_(axis, idx, src)
def _segment_sum(src, seg, n):
    out = torch.zeros((n,) + src.shape[1:], dtype=src.dtype)
    return out.index_add_(0, seg, src)
def _pad(x, pw):
    flat = tuple(v for pair in reversed(pw) for v in pair)
    return torch.nn.functional.pad(x, flat)

kernel_name = 'standalone__mha_block_cached__near_ridge'

def setup_inputs():

    _w = (lambda a: (lambda t: t.to(torch.bfloat16) if t.is_floating_point() else t)(torch.as_tensor(a)))
    B, Sq, D = 8, 192, 448
    H, Sk, Dh = 16, 1024, 256
    return {
        "x": _w((lambda *s: torch.randn(*s, dtype=torch.bfloat16) * 0.8)(B, Sq, D)),
        "k_cache": _w((lambda *s: torch.randn(*s, dtype=torch.bfloat16) * 0.8)(B, H, Sk, Dh)),
        "v_cache": _w((lambda *s: torch.randn(*s, dtype=torch.bfloat16) * 0.05)(B, H, Sk, Dh)),
        "wq": _w((lambda *s: torch.randn(*s, dtype=torch.bfloat16) * 0.8)(D, H * Dh) * (D ** -0.5)),
        "wo": _w((lambda *s: torch.randn(*s, dtype=torch.bfloat16) * 0.05)(H * Dh, D) * ((H * Dh) ** -0.5)),
    }

def reference(x, k_cache, v_cache, wq, wo):
    B, Sq, D = x.shape
    H, Dh = 16, 256
    q = (lambda x, *d: x.permute(*d))((x @ wq).reshape(B, Sq, H, Dh), 0, 2, 1, 3)
    scores = torch.einsum("bhqd,bhkd->bhqk", q, k_cache) * (Dh ** -0.5)
    p = _softmax(scores, axis=-1)
    o = torch.einsum("bhqk,bhkd->bhqd", p, v_cache)
    return (lambda x, *d: x.permute(*d))(o, 0, 2, 1, 3).reshape(B, Sq, H * Dh) @ wo
```

```cpp
#include <hip/hip_runtime.h>
#include <hip/hip_bf16.h>
typedef __hip_bfloat16 bf16;
constexpr int B=8, SQ=192, D=448, H=16, SK=1024, DH=256;
__device__ __forceinline__ float b2f(bf16 v){ return __bfloat162float(v);}
__global__ void k_qproj(const bf16* x, const bf16* wq, bf16* q){
  int n = blockIdx.x*256+threadIdx.x; int m = blockIdx.y;
  float acc=0; for(int k=0;k<D;++k) acc += b2f(x[m*D+k])*b2f(wq[(size_t)k*H*DH+n]);
  q[(size_t)m*H*DH+n]=__float2bfloat16(acc);
}
__global__ void k_attn(const bf16* q, const bf16* kc, const bf16* vc, bf16* o){
  __shared__ float s[SK]; __shared__ float qs[DH]; __shared__ float red[256];
  int row=blockIdx.x;
  int h=blockIdx.y; int b=row/SQ; int t=threadIdx.x;
  qs[t]=b2f(q[(size_t)row*H*DH+h*DH+t]); __syncthreads();
  const bf16* kb=kc+((size_t)(b*H+h))*SK*DH; const bf16* vb=vc+((size_t)(b*H+h))*SK*DH;
  float mx=-1e30f;
  for(int j=t;j<SK;j+=256){ float a=0; for(int d=0;d<DH;++d) a+=qs[d]*b2f(kb[(size_t)j*DH+d]); a*=0.0625f; s[j]=a; mx=fmaxf(mx,a);}
  red[t]=mx; __syncthreads(); for(int o2=128;o2>0;o2>>=1){ if(t<o2) red[t]=fmaxf(red[t],red[t+o2]); __syncthreads(); }
  mx=red[0]; __syncthreads(); float sum=0;
  for(int j=t;j<SK;j+=256){ float e=__expf(s[j]-mx); s[j]=e; sum+=e; }
  red[t]=sum; __syncthreads(); for(int o2=128;o2>0;o2>>=1){ if(t<o2) red[t]+=red[t+o2]; __syncthreads(); }
  sum=red[0]; float acc=0; for(int j=0;j<SK;++j) acc+=s[j]*b2f(vb[(size_t)j*DH+t]);
  o[(size_t)row*H*DH+h*DH+t]=__float2bfloat16(acc/sum);
}
__global__ void k_oproj(const bf16* o, const bf16* wo, bf16* out){
  int n=threadIdx.x; int m=blockIdx.x; if(n>=D) return;
  float acc=0; for(int k=0;k<H*DH;++k) acc+=b2f(o[(size_t)m*H*DH+k])*b2f(wo[(size_t)k*D+n]);
  out[m*D+n]=__float2bfloat16(acc);
}
extern "C" void kernel_launch(void* const* d_in, const int* in_sizes, int n_in, void* d_out, int out_size, void* d_ws, size_t ws_size, hipStream_t stream){
  const bf16* x=(const bf16*)d_in[0]; const bf16* kc=(const bf16*)d_in[1]; const bf16* vc=(const bf16*)d_in[2];
  const bf16* wq=(const bf16*)d_in[3]; const bf16* wo=(const bf16*)d_in[4];
  bf16* q=(bf16*)d_ws; bf16* o=q+(size_t)B*SQ*H*DH;
  k_qproj<<<dim3(16,B*SQ),256,0,stream>>>(x,wq,q);
  k_attn<<<dim3(B*SQ,H),256,0,stream>>>(q,kc,vc,o);
  k_oproj<<<B*SQ,448,0,stream>>>(o,wo,(bf16*)d_out);
}
```

```cpp
#include <hip/hip_runtime.h>
#include <hip/hip_bf16.h>
#include <stdint.h>
#include <utility>

typedef __bf16 bf16x8 __attribute__((ext_vector_type(8)));
typedef __bf16 bf16x4 __attribute__((ext_vector_type(4)));
typedef __bf16 bf16x2 __attribute__((ext_vector_type(2)));
typedef float f32x16 __attribute__((ext_vector_type(16)));
typedef float f32x4 __attribute__((ext_vector_type(4)));
typedef float f32x2 __attribute__((ext_vector_type(2)));
typedef unsigned u32x4 __attribute__((ext_vector_type(4)));
typedef unsigned u32x2 __attribute__((ext_vector_type(2)));
#define DI __device__ __forceinline__
#define LDS_AS __attribute__((address_space(3)))
#define GAP() __builtin_amdgcn_sched_barrier(0)
template <int... Is> using iseq = std::integer_sequence<int, Is...>;
template <int N> using make_iseq = std::make_integer_sequence<int, N>;

constexpr int NB = 8, SQ = 192, DM = 448, NH = 16, SK = 1024, DH = 256, HD = NH * DH;
constexpr int NRING = 24;
constexpr int NPIECE = 312;
constexpr unsigned LDS_RING = 0, LDS_P = 4 * NRING * 1024, LDS_MX = LDS_P + 49152, LDS_SCR = LDS_MX + 3072, LDS_TOTAL = 163840;
constexpr float SCL = 0.0625f * 1.4426950408889634f;
constexpr float THR = 8.0f;
constexpr int NQA = 40;

struct Piece { int type, tile, p; };
constexpr Piece piece_of(int idx) {
  if (idx < 56) return Piece{0, idx >> 1, idx & 1};
  int j = idx - 56;
  if (j < 32) return Piece{1, j >> 4, j & 15};
  j -= 32;
  if (j < 192) { int blk = j >> 5, r = j & 31; return r < 16 ? Piece{1, blk + 2, r} : Piece{2, blk, r - 16}; }
  j -= 192;
  if (j < 32) return Piece{2, 6 + (j >> 4), j & 15};
  return Piece{3, 0, 0};
}
constexpr int imin(int a, int b) { return a < b ? a : b; }

struct Ctx {
  __amdgpu_buffer_rsrc_t srdK, srdV, srdW, srdX;
  unsigned voffK[2], voffV, voffW;
  unsigned sK, sV, sW;
  unsigned ringM0;
  unsigned vK, vV, vP, vPw, vMx, vMxw;
  bf16x8 q[48];
  f32x16 o[2][3];
  f32x16 s[2][3];
  bf16x8 kf[2][4], vf[2][2], pf[2][3];
  float m[3], l[3], mc[3], alpha[3], mxr[3]; f32x4 mxv[3]; bool resc;
};

DI unsigned cvtpk(float lo, float hi) { f32x2 v = {lo, hi}; bf16x2 b = __builtin_convertvector(v, bf16x2); return __builtin_bit_cast(unsigned, b); }
DI float max3f(float a, float b, float c) { float r; asm("v_max3_f32 %0, %1, %2, %3" : "=v"(r) : "v"(a), "v"(b), "v"(c)); return r; }
DI float max2f(float a, float b) { float r; asm("v_max_f32_e32 %0, %1, %2" : "=v"(r) : "v"(a), "v"(b)); return r; }
DI float fadd_s(float a, float b) { float r; asm("v_add_f32_e32 %0, %1, %2" : "=v"(r) : "v"(a), "v"(b)); return r; }
template <int N> DI void wait_vm() { asm volatile("s_waitcnt vmcnt(%0)" :: "i"(N) : "memory"); }
DI void barrier() { asm volatile("s_waitcnt lgkmcnt(0)\n\ts_barrier" ::: "memory"); }
DI void dma(unsigned m0, unsigned voff, __amdgpu_buffer_rsrc_t srd, unsigned soff) {
  asm volatile("s_mov_b32 m0, %0\n\ts_nop 0\n\tbuffer_load_dwordx4 %1, %2, %3 offen lds" :: "s"(m0), "v"(voff), "s"(srd), "s"(soff) : "memory");
}
DI void dma_dep(unsigned m0, unsigned voff, __amdgpu_buffer_rsrc_t srd, unsigned soff, unsigned dep) {
  asm volatile("s_mov_b32 m0, %0\n\ts_nop 0\n\tbuffer_load_dwordx4 %1, %2, %3 offen lds" :: "s"(m0), "v"(voff), "s"(srd), "s"(soff), "v"(dep) : "memory");
}
DI void dma_dep_nt(unsigned m0, unsigned voff, __amdgpu_buffer_rsrc_t srd, unsigned soff, unsigned dep) {
  asm volatile("s_mov_b32 m0, %0\n\ts_nop 0\n\tbuffer_load_dwordx4 %1, %2, %3 offen nt lds" :: "s"(m0), "v"(voff), "s"(srd), "s"(soff), "v"(dep) : "memory");
}
template <int IDX> DI void issue_piece(const Ctx& c, unsigned dep) {
  constexpr Piece pc = piece_of(IDX);
  if constexpr (pc.type != 3) {
    const unsigned m0 = c.ringM0 + (unsigned)(IDX % NRING) * 1024u;
    if constexpr (pc.type == 0) dma_dep(m0, c.voffW, c.srdW, c.sW + (unsigned)(16 * pc.tile + 8 * pc.p) * 8192u, dep);
    else if constexpr (pc.type == 1) dma_dep_nt(m0, c.voffK[pc.p & 1], c.srdK, c.sK + (unsigned)(128 * pc.tile + 8 * (pc.p & 3)) * 512u + (unsigned)(pc.p >> 2) * 128u, dep);
    else dma_dep_nt(m0, c.voffV, c.srdV, c.sV + (unsigned)(128 * pc.tile + 16 * (pc.p >> 1) + 4 * (pc.p & 1)) * 512u, dep);
  }
}
DI bf16x8 lds_b128(unsigned addr) { return *(const LDS_AS bf16x8*)(uintptr_t)addr; }
DI bf16x4 lds_tr(unsigned addr) { return __builtin_amdgcn_ds_read_tr16_b64_v4bf16((LDS_AS bf16x4*)(uintptr_t)addr); }
DI unsigned dep8(const bf16x8& v) { return __builtin_bit_cast(u32x4, v)[3]; }

template <int QI, bool ZERO> DI void mfma_s(f32x16& s, const bf16x8& kf, const bf16x8& qf) {
  if constexpr (QI < NQA) {
    if constexpr (ZERO) asm volatile("v_mfma_f32_32x32x16_bf16 %0, %1, %2, 0" : "=&v"(s) : "v"(kf), "a"(qf));
    else                asm volatile("v_mfma_f32_32x32x16_bf16 %0, %1, %2, %0" : "+v"(s) : "v"(kf), "a"(qf));
  } else {
    if constexpr (ZERO) asm volatile("v_mfma_f32_32x32x16_bf16 %0, %1, %2, 0" : "=&v"(s) : "v"(kf), "v"(qf));
    else                asm volatile("v_mfma_f32_32x32x16_bf16 %0, %1, %2, %0" : "+v"(s) : "v"(kf), "v"(qf));
  }
}
template <bool ZERO> DI void mfma_o(f32x16& o, const bf16x8& vf, const bf16x8& pf) {
  if constexpr (ZERO) asm volatile("v_mfma_f32_32x32x16_bf16 %0, %1, %2, 0" : "=&a"(o) : "v"(vf), "v"(pf));
  else                asm volatile("v_mfma_f32_32x32x16_bf16 %0, %1, %2, %0" : "+a"(o) : "v"(vf), "v"(pf));
}

template <int I0, int S> DI void rd_k(Ctx& c, int buf) { c.kf[buf][S] = lds_b128((c.vK ^ (32u * S)) + (unsigned)(I0 % NRING) * 1024u); }
template <int I0, int DT> DI void rd_v(Ctx& c, int buf) {
  bf16x4 lo = lds_tr((c.vV ^ (64u * DT)) + (unsigned)(I0 % NRING) * 1024u);
  bf16x4 hi = lds_tr((c.vV ^ (64u * DT)) + (unsigned)(I0 % NRING) * 1024u + 512u);
  c.vf[buf][DT] = __builtin_shufflevector(lo, hi, 0, 1, 2, 3, 4, 5, 6, 7);
}
template <int BUF, int KS, int QT> DI void rd_p(Ctx& c, int buf) { c.pf[buf][QT] = lds_b128((c.vP ^ (32u * KS)) + (unsigned)(QT * 8192 + BUF * 24576)); }
template <int I0, int G, int ISSUED> DI void wait_gran() { static_assert(imin(NPIECE, ISSUED) >= I0 + G, "not issued yet"); wait_vm<imin(NPIECE, ISSUED) - (I0 + G)>(); }
constexpr int base_of(int T) { return (T == -1) ? 56 : (T == 0) ? 72 : (T <= 6) ? 88 + 32 * (T - 1) : 280 + 16 * (T - 7); }
constexpr int issued_at(int T, int G) { return NRING + base_of(T) + (G + 1) / 3; }

constexpr int ik_of(int T) { return (T == -1) ? 56 : (T == 0) ? 72 : 88 + 32 * (T - 1); }
constexpr int iv_of(int T) { return (T <= 6) ? 88 + 32 * (T - 1) + 16 : 280 + 16 * (T - 7); }

template <int X, int E> DI void sm_exp(Ctx& c) { c.s[X][E >> 4][E & 15] = __builtin_amdgcn_exp2f(fmaf(c.s[X][E >> 4][E & 15], SCL, c.mc[E >> 4])); }
template <int X, int E> DI void sm_add(Ctx& c) { c.l[E >> 4] = fadd_s(c.l[E >> 4], c.s[X][E >> 4][E & 15]); }
template <int X, int BUF, int CH> DI void sm_pack(Ctx& c) {
  constexpr int qt = CH >> 1, ss = CH & 1;
  const f32x16& p = c.s[X][qt];
  u32x4 v = {cvtpk(p[8 * ss], p[8 * ss + 1]), cvtpk(p[8 * ss + 2], p[8 * ss + 3]), cvtpk(p[8 * ss + 4], p[8 * ss + 5]), cvtpk(p[8 * ss + 6], p[8 * ss + 7])};
  *(LDS_AS u32x4*)(uintptr_t)((c.vPw ^ (32u * ss)) + (unsigned)(qt * 8192 + BUF * 24576)) = v;
}
template <int X, int QT, int I> DI void mx_step(Ctx& c) {
  if constexpr (I == 0) c.mxr[QT] = max2f(c.s[X][QT][0], c.s[X][QT][1]);
  else c.mxr[QT] = max3f(c.mxr[QT], c.s[X][QT][2 * I], c.s[X][QT][2 * I + 1]);
}
template <int PAR, int QT> DI void mx_fin(Ctx& c) {
  auto pr = __builtin_amdgcn_permlane32_swap(__float_as_uint(c.mxr[QT]), __float_as_uint(c.mxr[QT]), false, false);
  const float mx = max2f(__uint_as_float(pr[0]), __uint_as_float(pr[1]));
  *(LDS_AS float*)(uintptr_t)(c.vMxw + (unsigned)(PAR * 1536 + QT * 512)) = mx;
}

template <int T, int G> DI void gap(Ctx& c) {
  constexpr bool HAS_S = T < 7, HAS_SM = T >= 0 && T < 8, HAS_PV = T >= 1;
  constexpr int NS = HAS_S ? 48 : 0, NG = NS + (HAS_PV ? 48 : 0);
  constexpr int X = T & 1, XN = X ^ 1;
  constexpr int IK = ik_of(T), IV = iv_of(T);
  if constexpr (G < NS) {
    constexpr int g = G / 12, s = (G % 12) / 3, qt = G % 3;
    mfma_s<qt * 16 + 4 * g + s, (g == 0 && s == 0)>(c.s[XN][qt], c.kf[g & 1][s], c.q[qt * 16 + 4 * g + s]);
  } else {
    constexpr int P = G - NS, ks = P / 6, dt = (P % 6) / 3, qt = P % 3;
    mfma_o<(T == 1 && ks == 0)>(c.o[dt][qt], c.vf[ks & 1][dt], c.pf[ks & 1][qt]);
  }
  constexpr int ISS = issued_at(T, G);
  if constexpr (G < NS) {
    constexpr int g = G / 12, j = G % 12;
    if constexpr (g < 3) {
      constexpr int I0 = IK + 4 * (g + 1);
      if constexpr (j == 0) { wait_gran<I0, 4, ISS>(); rd_k<I0, 0>(c, (g + 1) & 1); }
      if constexpr (j == 2) rd_k<I0, 1>(c, (g + 1) & 1);
      if constexpr (j == 3) rd_k<I0, 2>(c, (g + 1) & 1);
      if constexpr (j == 5) rd_k<I0, 3>(c, (g + 1) & 1);
    } else if constexpr (HAS_PV) {
      if constexpr (j == 0) { wait_gran<IV, 2, ISS>(); rd_v<IV, 0>(c, 0); }
      if constexpr (j == 2) rd_v<IV, 1>(c, 0);
      if constexpr (j == 3) rd_p<(T - 1) & 1, 0, 0>(c, 0);
      if constexpr (j == 5) rd_p<(T - 1) & 1, 0, 1>(c, 0);
      if constexpr (j == 6) rd_p<(T - 1) & 1, 0, 2>(c, 0);
    } else {
      constexpr int I0 = ik_of(T + 1);
      if constexpr (j == 0) { wait_gran<I0, 4, ISS>(); rd_k<I0, 0>(c, 0); }
      if constexpr (j == 2) rd_k<I0, 1>(c, 0);
      if constexpr (j == 3) rd_k<I0, 2>(c, 0);
      if constexpr (j == 5) rd_k<I0, 3>(c, 0);
    }
    if constexpr (G % 3 == 1) issue_piece<base_of(T) + NRING + G / 3>(c, dep8(c.kf[g & 1][3]));
  } else {
    constexpr int P = G - NS, ks = P / 6, j = P % 6;
    if constexpr (ks < 7) {
      constexpr int I0 = IV + 2 * (ks + 1);
      if constexpr (j == 0) { wait_gran<I0, 2, ISS>(); rd_v<I0, 0>(c, (ks + 1) & 1); }
      if constexpr (j == 2) { rd_v<I0, 1>(c, (ks + 1) & 1); }
      if constexpr (j == 3) { rd_p<(T - 1) & 1, ks + 1, 0>(c, (ks + 1) & 1); rd_p<(T - 1) & 1, ks + 1, 1>(c, (ks + 1) & 1); }
      if constexpr (j == 4) { rd_p<(T - 1) & 1, ks + 1, 2>(c, (ks + 1) & 1); }
    } else if constexpr (T < 6) {
      constexpr int I0 = ik_of(T + 1);
      if constexpr (j == 0) { wait_gran<I0, 4, ISS>(); rd_k<I0, 0>(c, 0); }
      if constexpr (j == 2) rd_k<I0, 1>(c, 0);
      if constexpr (j == 3) rd_k<I0, 2>(c, 0);
      if constexpr (j == 5) rd_k<I0, 3>(c, 0);
    } else if constexpr (T < 8) {
      constexpr int I0 = iv_of(T + 1);
      if constexpr (j == 0) { wait_gran<I0, 2, ISS>(); rd_v<I0, 0>(c, 0); }
      if constexpr (j == 2) rd_v<I0, 1>(c, 0);
    }
    if constexpr (G % 3 == 1) issue_piece<base_of(T) + NRING + G / 3>(c, dep8(c.vf[ks & 1][1]));
  }
  if constexpr (HAS_SM && T >= 1) {
    if constexpr (G == 0) {
#pragma unroll
      for (int qt = 0; qt < 3; ++qt) c.mxv[qt] = *(const LDS_AS f32x4*)(uintptr_t)(c.vMx + (unsigned)((T & 1) * 1536 + qt * 512));
    }
    if constexpr (G == 2) {
      bool need = false;
#pragma unroll
      for (int qt = 0; qt < 3; ++qt) { c.mxr[qt] = max2f(max3f(c.mxv[qt][0], c.mxv[qt][1], c.mxv[qt][2]), c.mxv[qt][3]); need |= (c.mxr[qt] - c.m[qt]) * SCL > THR; }
      c.resc = __any(need);
      if (__builtin_expect(c.resc, 0)) {
#pragma unroll
        for (int qt = 0; qt < 3; ++qt) { const float mn = fmaxf(c.m[qt], c.mxr[qt]); c.alpha[qt] = __builtin_amdgcn_exp2f((c.m[qt] - mn) * SCL); c.m[qt] = mn; c.l[qt] *= c.alpha[qt]; c.mc[qt] = -mn * SCL; }
      }
    }
  }
  if constexpr (HAS_SM) {
    constexpr int SM0 = 3, SM1 = (NG == 96) ? 84 : 44;
    constexpr int e_lo = (G <= SM0) ? 0 : (G >= SM1) ? 48 : ((G - SM0) * 48) / (SM1 - SM0);
    constexpr int e_hi = (G + 1 <= SM0) ? 0 : (G + 1 >= SM1) ? 48 : ((G + 1 - SM0) * 48) / (SM1 - SM0);
    constexpr int p_lo = (G - 1 <= SM0) ? 0 : (G - 1 >= SM1) ? 48 : ((G - 1 - SM0) * 48) / (SM1 - SM0);
    constexpr int p_hi = e_lo;
    if constexpr (p_lo + 0 < p_hi) sm_add<X, p_lo + 0>(c);
    if constexpr (p_lo + 1 < p_hi) sm_add<X, p_lo + 1>(c);
    if constexpr (e_lo + 0 < e_hi) sm_exp<X, e_lo + 0>(c);
    if constexpr (e_lo + 1 < e_hi) sm_exp<X, e_lo + 1>(c);
    if constexpr ((p_hi >> 3) > (p_lo >> 3)) sm_pack<X, T & 1, (p_lo >> 3)>(c);
  }
  if constexpr (HAS_S && HAS_PV) {
    constexpr int M0 = 52;
    if constexpr (G >= M0 && G < M0 + 24) { constexpr int i = G - M0; mx_step<XN, i / 8, i % 8>(c); }
    if constexpr (G >= M0 + 9 && G < M0 + 9 + 24 && ((G - M0 - 9) % 8) == 0) mx_fin<(T + 1) & 1, (G - M0 - 9) / 8>(c);
  }
  GAP();
}
template <int T, int... Gs> DI void gaps(Ctx& c, iseq<Gs...>) { (gap<T, Gs>(c), ...); }
template <int X, int PAR, int... Is> DI void mx_all(Ctx& c, iseq<Is...>) { (mx_step<X, Is / 8, Is % 8>(c), ...); mx_fin<PAR, 0>(c); mx_fin<PAR, 1>(c); mx_fin<PAR, 2>(c); }

template <int T> DI void interval(Ctx& c) {
  constexpr bool HAS_S = T < 7, HAS_PV = T >= 1;
  constexpr int NG = (HAS_S ? 48 : 0) + (HAS_PV ? 48 : 0);
  if constexpr (T >= 7) {
    rd_p<(T - 1) & 1, 0, 0>(c, 0); rd_p<(T - 1) & 1, 0, 1>(c, 0); rd_p<(T - 1) & 1, 0, 2>(c, 0);
  }
  c.resc = false;
  gaps<T>(c, make_iseq<NG>{});
  if constexpr (HAS_PV && T <= 7) {
    if (__builtin_expect(c.resc, 0)) {
      asm volatile("s_nop 7\n\ts_nop 7" : "+a"(c.o[0][0]), "+a"(c.o[0][1]), "+a"(c.o[0][2]), "+a"(c.o[1][0]), "+a"(c.o[1][1]), "+a"(c.o[1][2]));
#pragma unroll
      for (int dt = 0; dt < 2; ++dt)
#pragma unroll
        for (int qt = 0; qt < 3; ++qt)
#pragma unroll
          for (int r = 0; r < 16; ++r) c.o[dt][qt][r] *= c.alpha[qt];
      asm volatile("s_nop 1" : "+a"(c.o[0][0]), "+a"(c.o[0][1]), "+a"(c.o[0][2]), "+a"(c.o[1][0]), "+a"(c.o[1][1]), "+a"(c.o[1][2]));
    }
  }
  if constexpr (HAS_S && !HAS_PV) {
    asm volatile("s_nop 7\n\ts_nop 7" : "+v"(c.s[(T & 1) ^ 1][0]), "+v"(c.s[(T & 1) ^ 1][1]), "+v"(c.s[(T & 1) ^ 1][2]));
    mx_all<(T & 1) ^ 1, (T + 1) & 1>(c, make_iseq<24>{});
  }
  if constexpr (T < 8) barrier();
  GAP();
}

template <int Gr, int J> DI void qgap(Ctx& c) {
  constexpr int dt = J / 3, qt = J % 3;
  constexpr int ISS = NRING + 2 * Gr + (J > 1) + (J > 4);
  mfma_o<Gr == 0>(c.o[dt][qt], c.vf[Gr & 1][dt], c.pf[Gr & 1][qt]);
  if constexpr (Gr + 1 < 28) {
    constexpr int G1 = Gr + 1, cc1 = G1 >> 3, ks1 = G1 & 7;
    if constexpr (J == 0) { wait_gran<2 * G1, 2, ISS>(); rd_v<2 * G1, 0>(c, G1 & 1); }
    if constexpr (J == 2) rd_v<2 * G1, 1>(c, G1 & 1);
    if constexpr (ks1 != 0) {
      if constexpr (J == 3) { rd_p<cc1 & 1, ks1, 0>(c, G1 & 1); rd_p<cc1 & 1, ks1, 1>(c, G1 & 1); }
      if constexpr (J == 2) { rd_p<cc1 & 1, ks1, 2>(c, G1 & 1); }
    }
  } else {
    if constexpr (J == 0) { wait_gran<56, 4, ISS>(); rd_k<56, 0>(c, 0); }
    if constexpr (J == 2) rd_k<56, 1>(c, 0);
    if constexpr (J == 3) rd_k<56, 2>(c, 0);
    if constexpr (J == 5) rd_k<56, 3>(c, 0);
  }
  if constexpr (J == 1 || J == 4) issue_piece<2 * Gr + NRING + (J == 4)>(c, dep8(c.vf[Gr & 1][1]));
  GAP();
}
template <int Gr, int... Js> DI void qgran(Ctx& c, iseq<Js...>) { (qgap<Gr, Js>(c), ...); }

#define ST(i)
extern "C" __global__ __launch_bounds__(256, 1)
void attn_kernel(const unsigned short* __restrict__ x, const unsigned short* __restrict__ kc, const unsigned short* __restrict__ vc,
                 const unsigned short* __restrict__ wq, const unsigned short* __restrict__ wo, unsigned short* __restrict__ obuf,
                 unsigned short* __restrict__ wot, unsigned* __restrict__ cnt, unsigned long long* __restrict__ dbg) {
  extern __shared__ __attribute__((aligned(16))) char lds[];
  const unsigned ldsb = (unsigned)(uintptr_t)(LDS_AS char*)lds;
  const unsigned tid = threadIdx.x, lane = tid & 63u, l32 = lane & 31u, hi = lane >> 5;
  const unsigned w = __builtin_amdgcn_readfirstlane(tid >> 6);
  const unsigned bid = blockIdx.x, b = bid & 7u, idx = bid >> 3, h = idx >> 1, half = idx & 1u;
  const unsigned row0 = b * SQ + half * 96u;
  Ctx c;
  c.srdK = __builtin_amdgcn_make_buffer_rsrc((void*)(kc + (size_t)(b * NH + h) * SK * DH), (short)0, SK * DH * 2, 0x00020000);
  c.srdV = __builtin_amdgcn_make_buffer_rsrc((void*)(vc + (size_t)(b * NH + h) * SK * DH), (short)0, SK * DH * 2, 0x00020000);
  c.srdW = __builtin_amdgcn_make_buffer_rsrc((void*)wq, (short)0, DM * HD * 2, 0x00020000);
  c.srdX = __builtin_amdgcn_make_buffer_rsrc((void*)x, (short)0, NB * SQ * DM * 2, 0x00020000);
  {
    const unsigned a = lane >> 3, p7 = lane & 7u;
    c.voffK[0] = a * 512u + ((p7 ^ (a >> 1)) * 16u);
    c.voffK[1] = a * 512u + ((p7 ^ (a >> 1) ^ 4u) * 16u);
    c.voffV = ((a & 3u) + 8u * (a >> 2)) * 512u + ((p7 ^ ((a & 2u) << 1)) * 16u);
    c.voffW = a * 8192u + ((p7 ^ ((a & 2u) << 1)) * 16u);
    c.sK = w * 32u * 512u; c.sV = w * 128u; c.sW = h * 512u + w * 128u;
    c.ringM0 = ldsb + LDS_RING + w * (NRING * 1024u);
    c.vK = c.ringM0 + l32 * 128u + ((hi ^ ((l32 >> 1) & 7u)) * 16u);
    const unsigned g16 = lane >> 4, dblk = g16 & 1u, i16 = lane & 15u, q4 = i16 >> 2, p4 = i16 & 3u;
    c.vV = c.ringM0 + (8u * hi + q4) * 128u + (((2u * dblk + (p4 >> 1)) ^ ((q4 >> 1) << 2)) * 16u) + 8u * (p4 & 1u);
    c.vP = ldsb + LDS_P + l32 * 256u + ((hi ^ (l32 & 15u)) * 16u);
    c.vPw = ldsb + LDS_P + l32 * 256u + (((4u * w + hi) ^ (l32 & 15u)) * 16u);
    c.vMx = ldsb + LDS_MX + l32 * 16u; c.vMxw = c.vMx + w * 4u;
  }
  auto load_x = [&](int cc) {
#pragma unroll
    for (int i = 0; i < 6; ++i) {
      const unsigned pc = 6u * w + i;
      const unsigned voff = (lane >> 4) * 896u + ((((lane & 15u) ^ (lane >> 4)) ^ (4u * (pc & 3u))) * 16u);
      dma(ldsb + LDS_P + (unsigned)(cc & 1) * 24576u + pc * 1024u, voff, c.srdX, (row0 + 4u * pc) * 896u + (unsigned)cc * 256u);
    }
  };
  ST(0);
  if (bid == 0 && tid < 64) cnt[tid] = 0u;
  {
    auto srdO = __builtin_amdgcn_make_buffer_rsrc((void*)wo, (short)0, HD * DM * 2, 0x00020000);
#pragma unroll
    for (int i = 0; i < 4; ++i) {
      const unsigned ch = (4u * w + i) * 64u + lane, r = ch / 28u, cc = ch - 28u * r;
      const unsigned voff = (r < 32u) ? r * 896u + cc * 16u : 0xFFFFFF00u;
      dma(ldsb + LDS_MX + (4u * w + i) * 1024u, voff, srdO, (bid >> 1) * 32u * 896u + (bid & 1u) * 448u);
    }
  }
  load_x(0);
  [&]<int... Is>(iseq<Is...>) { (issue_piece<Is>(c, 0u), ...); }(make_iseq<NRING>{});
  load_x(1);
  wait_vm<NRING + 6>();
  barrier();
  ST(1);
  {
    const unsigned g16 = lane >> 4, i16 = lane & 15u, q4 = i16 >> 2, p4 = i16 & 3u;
#pragma unroll
    for (int i = 0; i < 4; ++i) {
      const unsigned nt = w + 4u * i;
      if (nt < 14u) {
        const unsigned a = ldsb + LDS_MX + (8u * g16 + q4) * 448u + nt * 32u + p4 * 8u;
        bf16x4 lo = lds_tr(a), hi4 = lds_tr(a + 4u * 448u);
        bf16x8 v = __builtin_shufflevector(lo, hi4, 0, 1, 2, 3, 4, 5, 6, 7);
        *(bf16x8*)(wot + ((size_t)((bid >> 1) * 28u + (bid & 1u) * 14u + nt) * 64u + lane) * 8u) = v;
      }
    }
  }
  wait_gran<0, 2, NRING>(); rd_v<0, 0>(c, 0); rd_v<0, 1>(c, 0);
  rd_p<0, 0, 0>(c, 0); rd_p<0, 0, 1>(c, 0); rd_p<0, 0, 2>(c, 0);
  GAP();
  [&]<int... Gs>(iseq<Gs...>) {
    ([&] {
      qgran<Gs>(c, make_iseq<6>{});
      if constexpr ((Gs & 7) == 7) {
        constexpr int cc1 = (Gs + 1) >> 3;
        wait_vm<14>();
        barrier();
        rd_p<cc1 & 1, 0, 0>(c, (Gs + 1) & 1); rd_p<cc1 & 1, 0, 1>(c, (Gs + 1) & 1); rd_p<cc1 & 1, 0, 2>(c, (Gs + 1) & 1);
        if constexpr (cc1 + 1 < 4) load_x(cc1 + 1);
        GAP();
      }
    }(), ...);
  }(make_iseq<28>{});
  barrier();
  ST(2);
  asm volatile("s_nop 7\n\ts_nop 7" : "+a"(c.o[0][0]), "+a"(c.o[0][1]), "+a"(c.o[0][2]), "+a"(c.o[1][0]), "+a"(c.o[1][1]), "+a"(c.o[1][2]));
#pragma unroll
  for (int qt = 0; qt < 3; ++qt)
#pragma unroll
    for (int dt = 0; dt < 2; ++dt)
#pragma unroll
      for (int g = 0; g < 4; ++g) {
        u32x2 v = {cvtpk(c.o[dt][qt][4 * g], c.o[dt][qt][4 * g + 1]), cvtpk(c.o[dt][qt][4 * g + 2], c.o[dt][qt][4 * g + 3])};
        const unsigned ci = 8u * w + 4u * dt + g;
        *(LDS_AS u32x2*)(uintptr_t)(ldsb + LDS_P + (32u * qt + l32) * 512u + ((ci ^ (l32 & 15u)) * 16u) + 8u * hi) = v;
      }
  barrier();
#pragma unroll
  for (int qt = 0; qt < 3; ++qt)
#pragma unroll
    for (int ks = 0; ks < 16; ++ks)
      c.q[qt * 16 + ks] = lds_b128(ldsb + LDS_P + (32u * qt + l32) * 512u + (((2u * ks + hi) ^ (l32 & 15u)) * 16u));
  GAP();
  ST(3);
  interval<-1>(c);
  ST(4);
  {
#pragma unroll
    for (int qt = 0; qt < 3; ++qt) {
      f32x4 v = *(const LDS_AS f32x4*)(uintptr_t)(c.vMx + (unsigned)(qt * 512));
      c.m[qt] = fmaxf(fmaxf(v[0], v[1]), fmaxf(v[2], v[3])); c.l[qt] = 0.f; c.mc[qt] = -c.m[qt] * SCL;
    }
  }
  GAP();
  interval<0>(c); ST(5); interval<1>(c); ST(6); interval<2>(c); ST(7); interval<3>(c); ST(8); interval<4>(c); ST(9);
  interval<5>(c); ST(10); interval<6>(c); ST(11); interval<7>(c); ST(12); interval<8>(c); ST(13);
  {
#pragma unroll
    for (int qt = 0; qt < 3; ++qt) {
      auto pr = __builtin_amdgcn_permlane32_swap(__float_as_uint(c.l[qt]), __float_as_uint(c.l[qt]), false, false);
      const float lt = __uint_as_float(pr[0]) + __uint_as_float(pr[1]);
      *(LDS_AS float*)(uintptr_t)(c.vMxw + (unsigned)(qt * 512)) = lt;
    }
    barrier();
    asm volatile("s_nop 7\n\ts_nop 7" : "+a"(c.o[0][0]), "+a"(c.o[0][1]), "+a"(c.o[0][2]), "+a"(c.o[1][0]), "+a"(c.o[1][1]), "+a"(c.o[1][2]));
#pragma unroll
    for (int qt = 0; qt < 3; ++qt) {
      f32x4 v = *(const LDS_AS f32x4*)(uintptr_t)(c.vMx + (unsigned)(qt * 512));
      const float inv = 1.0f / ((v[0] + v[1]) + (v[2] + v[3]));
#pragma unroll
      for (int dt = 0; dt < 2; ++dt)
#pragma unroll
        for (int g = 0; g < 4; ++g) {
          u32x2 o2 = {cvtpk(c.o[dt][qt][4 * g] * inv, c.o[dt][qt][4 * g + 1] * inv), cvtpk(c.o[dt][qt][4 * g + 2] * inv, c.o[dt][qt][4 * g + 3] * inv)};
          const unsigned ci = 4u * dt + g;
          *(LDS_AS u32x2*)(uintptr_t)(c.ringM0 + (32u * qt + l32) * 128u + ((ci ^ (l32 & 7u)) * 16u) + 8u * hi) = o2;
        }
    }
    asm volatile("s_waitcnt lgkmcnt(0)" ::: "memory");
#pragma unroll
    for (int i = 0; i < 12; ++i) {
      const unsigned mt = i >> 1, ksl = i & 1, q = 16u * mt + (lane & 15u), ci = 4u * ksl + (lane >> 4);
      u32x4 v = *(const LDS_AS u32x4*)(uintptr_t)(c.ringM0 + q * 128u + ((ci ^ (q & 7u)) * 16u));
      *(u32x4*)(obuf + ((size_t)((row0 / 16u + mt) * 128u + 8u * h + 2u * w + ksl) * 64u + lane) * 8u) = v;
    }
  }
  ST(14);
}

#define MFMA16(a, b, c) __builtin_amdgcn_mfma_f32_16x16x32_bf16((a), (b), (c), 0, 0, 0)
constexpr int OPD = 6;
extern "C" __global__ __launch_bounds__(512, 2)
void oproj_kernel(const unsigned short* __restrict__ obuf, const unsigned short* __restrict__ wot, unsigned short* __restrict__ out) {
  extern __shared__ __attribute__((aligned(16))) char lds[];
  const unsigned tid = threadIdx.x, lane = tid & 63u;
  const unsigned w = __builtin_amdgcn_readfirstlane(tid >> 6);
  const unsigned bid = blockIdx.x, xcd = bid & 7u, j = bid >> 3;
  const unsigned ng = j % 7u, mg = xcd * 4u + j / 7u;
  const unsigned ks0 = w * 16u;
  const bf16x8* A = (const bf16x8*)obuf + ((size_t)(mg * 3u) * 128u + ks0) * 64u + lane;
  const bf16x8* Bm = (const bf16x8*)wot + ((size_t)ks0 * 28u + ng * 4u) * 64u + lane;
  f32x4 acc[3][4];
#pragma unroll
  for (int m = 0; m < 3; ++m)
#pragma unroll
    for (int n = 0; n < 4; ++n) acc[m][n] = f32x4{0.f, 0.f, 0.f, 0.f};
  bf16x8 af[OPD][3], bfr[OPD][4];
#pragma unroll
  for (int p = 0; p < OPD; ++p) {
#pragma unroll
    for (int m = 0; m < 3; ++m) af[p][m] = A[(m * 128 + p) * 64];
#pragma unroll
    for (int n = 0; n < 4; ++n) bfr[p][n] = Bm[(p * 28 + n) * 64];
  }
  GAP();
#pragma unroll
  for (int ks = 0; ks < 16; ++ks) {
#pragma unroll
    for (int m = 0; m < 3; ++m)
#pragma unroll
      for (int n = 0; n < 4; ++n) acc[m][n] = MFMA16(af[ks % OPD][m], bfr[ks % OPD][n], acc[m][n]);
    GAP();
    if (ks + OPD < 16) {
#pragma unroll
      for (int m = 0; m < 3; ++m) af[ks % OPD][m] = A[(m * 128 + ks + OPD) * 64];
#pragma unroll
      for (int n = 0; n < 4; ++n) bfr[ks % OPD][n] = Bm[((ks + OPD) * 28 + n) * 64];
    }
    GAP();
  }
  f32x4* L = (f32x4*)lds;
#pragma unroll
  for (int m = 0; m < 3; ++m)
#pragma unroll
    for (int n = 0; n < 4; ++n) L[(w * 12u + m * 4 + n) * 64u + lane] = acc[m][n];
  __syncthreads();
  for (unsigned it = tid; it < 768u; it += 512u) {
    f32x4 sum = L[it];
#pragma unroll
    for (int ww = 1; ww < 8; ++ww) sum += L[ww * 768u + it];
    const unsigned t = it >> 6, l = it & 63u, m = t >> 2, n = t & 3u;
    const unsigned row = mg * 48u + 16u * m + 4u * (l >> 4), col = ng * 64u + 16u * n + (l & 15u);
    const unsigned p01 = cvtpk(sum[0], sum[1]), p23 = cvtpk(sum[2], sum[3]);
    out[(size_t)(row + 0) * DM + col] = (unsigned short)(p01 & 0xFFFFu);
    out[(size_t)(row + 1) * DM + col] = (unsigned short)(p01 >> 16);
    out[(size_t)(row + 2) * DM + col] = (unsigned short)(p23 & 0xFFFFu);
    out[(size_t)(row + 3) * DM + col] = (unsigned short)(p23 >> 16);
  }
}

extern "C" void kernel_launch(void* const* d_in, const int* in_sizes, int n_in, void* d_out, int out_size, void* d_ws, size_t ws_size, hipStream_t stream) {
  const unsigned short* x = (const unsigned short*)d_in[0]; const unsigned short* kc = (const unsigned short*)d_in[1];
  const unsigned short* vc = (const unsigned short*)d_in[2]; const unsigned short* wq = (const unsigned short*)d_in[3];
  const unsigned short* wo = (const unsigned short*)d_in[4];
  char* ws = (char*)d_ws;
  unsigned short* obuf = (unsigned short*)ws;
  unsigned short* wot = (unsigned short*)(ws + (16u << 20));
  float* slab = (float*)(ws + (24u << 20));
  unsigned* cnt = (unsigned*)(ws + (40u << 20));
  (void)hipFuncSetAttribute((const void*)attn_kernel, hipFuncAttributeMaxDynamicSharedMemorySize, (int)LDS_TOTAL);
  (void)hipFuncSetAttribute((const void*)oproj_kernel, hipFuncAttributeMaxDynamicSharedMemorySize, 8 * 12 * 1024);
  attn_kernel<<<256, 256, LDS_TOTAL, stream>>>(x, kc, vc, wq, wo, obuf, wot, cnt, (unsigned long long*)(ws + (64u << 20)));
  oproj_kernel<<<224, 512, 8 * 12 * 1024, stream>>>(obuf, wot, (unsigned short*)d_out);
}
```

```cpp
#include <hip/hip_runtime.h>
#include <hip/hip_bf16.h>
#include <stdint.h>
#include <utility>

typedef __bf16 bf16x8 __attribute__((ext_vector_type(8)));
typedef __bf16 bf16x4 __attribute__((ext_vector_type(4)));
typedef __bf16 bf16x2 __attribute__((ext_vector_type(2)));
typedef float f32x16 __attribute__((ext_vector_type(16)));
typedef float f32x4 __attribute__((ext_vector_type(4)));
typedef float f32x2 __attribute__((ext_vector_type(2)));
typedef unsigned u32x4 __attribute__((ext_vector_type(4)));
typedef unsigned u32x2 __attribute__((ext_vector_type(2)));
#define DI __device__ __forceinline__
#define LDS_AS __attribute__((address_space(3)))
#define GAP() __builtin_amdgcn_sched_barrier(0)
template <int... Is> using iseq = std::integer_sequence<int, Is...>;
template <int N> using make_iseq = std::make_integer_sequence<int, N>;

constexpr int NB = 8, SQ = 192, DM = 448, NH = 16, SK = 1024, DH = 256, HD = NH * DH;
constexpr int NRING = 24;
constexpr int NPIECE = 312;
constexpr unsigned LDS_RING = 0, LDS_P = 4 * NRING * 1024, LDS_MX = LDS_P + 49152, LDS_SCR = LDS_MX + 3072, LDS_TOTAL = 163840;
constexpr float SCL = 0.0625f * 1.4426950408889634f;
constexpr float THR = 8.0f;
#define SM1_96 60
#define MX0 58
constexpr int NQA = 40;

struct Piece { int type, tile, p; };
constexpr Piece piece_of(int idx) {
  if (idx < 56) return Piece{0, idx >> 1, idx & 1};
  int j = idx - 56;
  if (j < 32) return Piece{1, j >> 4, j & 15};
  j -= 32;
  if (j < 192) { int blk = j >> 5, r = j & 31; return r < 16 ? Piece{1, blk + 2, r} : Piece{2, blk, r - 16}; }
  j -= 192;
  if (j < 32) return Piece{2, 6 + (j >> 4), j & 15};
  return Piece{3, 0, 0};
}
constexpr int imin(int a, int b) { return a < b ? a : b; }

struct Ctx {
  __amdgpu_buffer_rsrc_t srdK, srdV, srdW, srdX, srdO;
  unsigned voffO, sO;
  unsigned voffK[2], voffV, voffW;
  unsigned sK, sV, sW;
  unsigned ringM0;
  unsigned vK, vV, vP, vPw, vMx, vMxw;
  bf16x8 q[48];
  f32x16 o[2][3];
  f32x16 s[2][3];
  bf16x8 kf[2][4], vf[2][2], pf[2][3];
  float m[3], l[3], mc[3], alpha[3], mxr[3]; f32x4 mxv[3]; bool resc;
};

DI unsigned cvtpk(float lo, float hi) { f32x2 v = {lo, hi}; bf16x2 b = __builtin_convertvector(v, bf16x2); return __builtin_bit_cast(unsigned, b); }
DI float max3f(float a, float b, float c) { float r; asm("v_max3_f32 %0, %1, %2, %3" : "=v"(r) : "v"(a), "v"(b), "v"(c)); return r; }
DI float max2f(float a, float b) { float r; asm("v_max_f32_e32 %0, %1, %2" : "=v"(r) : "v"(a), "v"(b)); return r; }
DI float fadd_s(float a, float b) { float r; asm("v_add_f32_e32 %0, %1, %2" : "=v"(r) : "v"(a), "v"(b)); return r; }
template <int N> DI void wait_vm() { asm volatile("s_waitcnt vmcnt(%0)" :: "i"(N) : "memory"); }
DI void barrier() { asm volatile("s_waitcnt lgkmcnt(0)\n\ts_barrier" ::: "memory"); }
DI void dma(unsigned m0, unsigned voff, __amdgpu_buffer_rsrc_t srd, unsigned soff) {
  asm volatile("s_mov_b32 m0, %0\n\ts_nop 0\n\tbuffer_load_dwordx4 %1, %2, %3 offen lds" :: "s"(m0), "v"(voff), "s"(srd), "s"(soff) : "memory");
}
DI void dma_dep(unsigned m0, unsigned voff, __amdgpu_buffer_rsrc_t srd, unsigned soff, unsigned dep) {
  asm volatile("s_mov_b32 m0, %0\n\ts_nop 0\n\tbuffer_load_dwordx4 %1, %2, %3 offen lds" :: "s"(m0), "v"(voff), "s"(srd), "s"(soff), "v"(dep) : "memory");
}
template <unsigned M0OFF, unsigned SOFF> DI void dma_dep_nt(unsigned m0b, unsigned voff, __amdgpu_buffer_rsrc_t srd, unsigned sb, unsigned dep) {
  unsigned tmp;
  asm volatile("s_add_u32 m0, %1, %6\n\ts_add_u32 %0, %4, %7\n\tbuffer_load_dwordx4 %2, %3, %0 offen nt lds" : "=&s"(tmp) : "s"(m0b), "v"(voff), "s"(srd), "s"(sb), "v"(dep), "i"(M0OFF), "i"(SOFF) : "memory", "scc");
}
template <int IDX> DI void issue_piece(const Ctx& c, unsigned dep) {
  constexpr Piece pc = piece_of(IDX);
  if constexpr (pc.type != 3) {
    const unsigned m0 = c.ringM0 + (unsigned)(IDX % NRING) * 1024u;
    if constexpr (pc.type == 0) dma_dep(m0, c.voffW, c.srdW, c.sW + (unsigned)(16 * pc.tile + 8 * pc.p) * 8192u, dep);
    else if constexpr (pc.type == 1) dma_dep_nt<(unsigned)(IDX % NRING) * 1024u, (unsigned)(128 * pc.tile + 8 * (pc.p & 3)) * 512u + (unsigned)(pc.p >> 2) * 128u>(c.ringM0, c.voffK[pc.p & 1], c.srdK, c.sK, dep);
    else dma_dep_nt<(unsigned)(IDX % NRING) * 1024u, (unsigned)(128 * pc.tile + 16 * (pc.p >> 1) + 4 * (pc.p & 1)) * 512u>(c.ringM0, c.voffV, c.srdV, c.sV, dep);
  }
}
DI bf16x8 lds_b128(unsigned addr) { return *(const LDS_AS bf16x8*)(uintptr_t)addr; }
DI bf16x4 lds_tr(unsigned addr) { return __builtin_amdgcn_ds_read_tr16_b64_v4bf16((LDS_AS bf16x4*)(uintptr_t)addr); }
DI unsigned dep8(const bf16x8& v) { return __builtin_bit_cast(u32x4, v)[3]; }

template <int QI, bool ZERO> DI void mfma_s(f32x16& s, const bf16x8& kf, const bf16x8& qf) {
  if constexpr (QI < NQA) {
    if constexpr (ZERO) asm volatile("v_mfma_f32_32x32x16_bf16 %0, %1, %2, 0" : "=&v"(s) : "v"(kf), "a"(qf));
    else                asm volatile("v_mfma_f32_32x32x16_bf16 %0, %1, %2, %0" : "+v"(s) : "v"(kf), "a"(qf));
  } else {
    if constexpr (ZERO) asm volatile("v_mfma_f32_32x32x16_bf16 %0, %1, %2, 0" : "=&v"(s) : "v"(kf), "v"(qf));
    else                asm volatile("v_mfma_f32_32x32x16_bf16 %0, %1, %2, %0" : "+v"(s) : "v"(kf), "v"(qf));
  }
}
template <bool ZERO> DI void mfma_o(f32x16& o, const bf16x8& vf, const bf16x8& pf) {
  if constexpr (ZERO) asm volatile("v_mfma_f32_32x32x16_bf16 %0, %1, %2, 0" : "=&a"(o) : "v"(vf), "v"(pf));
  else                asm volatile("v_mfma_f32_32x32x16_bf16 %0, %1, %2, %0" : "+a"(o) : "v"(vf), "v"(pf));
}

template <int I0, int S> DI void rd_k(Ctx& c, int buf) { c.kf[buf][S] = lds_b128((c.vK ^ (32u * S)) + (unsigned)(I0 % NRING) * 1024u); }
template <int I0, int DT> DI void rd_v(Ctx& c, int buf) {
  bf16x4 lo = lds_tr((c.vV ^ (64u * DT)) + (unsigned)(I0 % NRING) * 1024u);
  bf16x4 hi = lds_tr((c.vV ^ (64u * DT)) + (unsigned)(I0 % NRING) * 1024u + 512u);
  c.vf[buf][DT] = __builtin_shufflevector(lo, hi, 0, 1, 2, 3, 4, 5, 6, 7);
}
template <int BUF, int KS, int QT> DI void rd_p(Ctx& c, int buf) { c.pf[buf][QT] = lds_b128((c.vP ^ (32u * KS)) + (unsigned)(QT * 8192 + BUF * 24576)); }
constexpr int NEXTRA = 8;
template <int I0, int G, int ISSUED, int YOUNGER = 0> DI void wait_gran() {
  static_assert(imin(NPIECE, ISSUED) >= I0 + G, "not issued yet");
  constexpr int n = imin(NPIECE, ISSUED) - (I0 + G) + ((I0 + G <= NRING) ? NEXTRA : 0) + YOUNGER;
  wait_vm<(n > 63 ? 63 : n)>();
}
constexpr int base_of(int T) { return (T == -1) ? 56 : (T == 0) ? 72 : (T <= 6) ? 88 + 32 * (T - 1) : 280 + 16 * (T - 7); }
constexpr int issued_at(int T, int G) { return NRING + base_of(T) + (G + 1) / 3; }

constexpr int ik_of(int T) { return (T == -1) ? 56 : (T == 0) ? 72 : 88 + 32 * (T - 1); }
constexpr int iv_of(int T) { return (T <= 6) ? 88 + 32 * (T - 1) + 16 : 280 + 16 * (T - 7); }

template <int X, int E> DI void sm_exp(Ctx& c) { c.s[X][E >> 4][E & 15] = __builtin_amdgcn_exp2f(fmaf(c.s[X][E >> 4][E & 15], SCL, c.mc[E >> 4])); }
template <int X, int E> DI void sm_add(Ctx& c) { c.l[E >> 4] = fadd_s(c.l[E >> 4], c.s[X][E >> 4][E & 15]); }
template <int X, int BUF, int CH> DI void sm_pack(Ctx& c) {
  constexpr int qt = CH >> 1, ss = CH & 1;
  const f32x16& p = c.s[X][qt];
  u32x4 v = {cvtpk(p[8 * ss], p[8 * ss + 1]), cvtpk(p[8 * ss + 2], p[8 * ss + 3]), cvtpk(p[8 * ss + 4], p[8 * ss + 5]), cvtpk(p[8 * ss + 6], p[8 * ss + 7])};
  *(LDS_AS u32x4*)(uintptr_t)((c.vPw ^ (32u * ss)) + (unsigned)(qt * 8192 + BUF * 24576)) = v;
}
template <int X, int QT, int I> DI void mx_step(Ctx& c) {
  if constexpr (I == 0) c.mxr[QT] = max2f(c.s[X][QT][0], c.s[X][QT][1]);
  else c.mxr[QT] = max3f(c.mxr[QT], c.s[X][QT][2 * I], c.s[X][QT][2 * I + 1]);
}
template <int PAR, int QT> DI void mx_fin(Ctx& c) {
  auto pr = __builtin_amdgcn_permlane32_swap(__float_as_uint(c.mxr[QT]), __float_as_uint(c.mxr[QT]), false, false);
  const float mx = max2f(__uint_as_float(pr[0]), __uint_as_float(pr[1]));
  *(LDS_AS float*)(uintptr_t)(c.vMxw + (unsigned)(PAR * 1536 + QT * 512)) = mx;
}

template <int T, int G> DI void gap(Ctx& c) {
  constexpr bool HAS_S = T < 7, HAS_SM = T >= 0 && T < 8, HAS_PV = T >= 1;
  constexpr int NS = HAS_S ? 48 : 0, NG = NS + (HAS_PV ? 48 : 0);
  constexpr int X = T & 1, XN = X ^ 1;
  constexpr int IK = ik_of(T), IV = iv_of(T);
  if constexpr (G < NS) {
    constexpr int g = G / 12, s = (G % 12) / 3, qt = G % 3;
    mfma_s<qt * 16 + 4 * g + s, (g == 0 && s == 0)>(c.s[XN][qt], c.kf[g & 1][s], c.q[qt * 16 + 4 * g + s]);
  } else {
    constexpr int P = G - NS, ks = P / 6, dt = (P % 6) / 3, qt = P % 3;
    mfma_o<(T == 1 && ks == 0)>(c.o[dt][qt], c.vf[ks & 1][dt], c.pf[ks & 1][qt]);
  }
  constexpr int ISS = issued_at(T, G);
  constexpr int YW = (T == 8) ? 4 : (T == 7 && G > 37) ? (G - 35) / 3 : 0;
  if constexpr (G < NS) {
    constexpr int g = G / 12, j = G % 12;
    if constexpr (g < 3) {
      constexpr int I0 = IK + 4 * (g + 1);
      if constexpr (j == 0) { wait_gran<I0, 4, ISS>(); rd_k<I0, 0>(c, (g + 1) & 1); }
      if constexpr (j == 2) rd_k<I0, 1>(c, (g + 1) & 1);
      if constexpr (j == 3) rd_k<I0, 2>(c, (g + 1) & 1);
      if constexpr (j == 5) rd_k<I0, 3>(c, (g + 1) & 1);
    } else if constexpr (HAS_PV) {
      if constexpr (j == 0) { wait_gran<IV, 2, ISS>(); rd_v<IV, 0>(c, 0); }
      if constexpr (j == 2) rd_v<IV, 1>(c, 0);
      if constexpr (j == 3) rd_p<(T - 1) & 1, 0, 0>(c, 0);
      if constexpr (j == 5) rd_p<(T - 1) & 1, 0, 1>(c, 0);
      if constexpr (j == 6) rd_p<(T - 1) & 1, 0, 2>(c, 0);
    } else {
      constexpr int I0 = ik_of(T + 1);
      if constexpr (j == 0) { wait_gran<I0, 4, ISS>(); rd_k<I0, 0>(c, 0); }
      if constexpr (j == 2) rd_k<I0, 1>(c, 0);
      if constexpr (j == 3) rd_k<I0, 2>(c, 0);
      if constexpr (j == 5) rd_k<I0, 3>(c, 0);
    }
    if constexpr (G % 3 == 1) issue_piece<base_of(T) + NRING + G / 3>(c, dep8(c.kf[g & 1][3]));
  } else {
    constexpr int P = G - NS, ks = P / 6, j = P % 6;
    if constexpr (ks < 7) {
      constexpr int I0 = IV + 2 * (ks + 1);
      if constexpr (j == 0) { wait_gran<I0, 2, ISS, YW>(); rd_v<I0, 0>(c, (ks + 1) & 1); }
      if constexpr (j == 2) { rd_v<I0, 1>(c, (ks + 1) & 1); }
      if constexpr (j == 3) { rd_p<(T - 1) & 1, ks + 1, 0>(c, (ks + 1) & 1); rd_p<(T - 1) & 1, ks + 1, 1>(c, (ks + 1) & 1); }
      if constexpr (j == 4) { rd_p<(T - 1) & 1, ks + 1, 2>(c, (ks + 1) & 1); }
    } else if constexpr (T < 6) {
      constexpr int I0 = ik_of(T + 1);
      if constexpr (j == 0) { wait_gran<I0, 4, ISS>(); rd_k<I0, 0>(c, 0); }
      if constexpr (j == 2) rd_k<I0, 1>(c, 0);
      if constexpr (j == 3) rd_k<I0, 2>(c, 0);
      if constexpr (j == 5) rd_k<I0, 3>(c, 0);
    } else if constexpr (T < 8) {
      constexpr int I0 = iv_of(T + 1);
      if constexpr (j == 0) { wait_gran<I0, 2, ISS, YW>(); rd_v<I0, 0>(c, 0); }
      if constexpr (j == 2) rd_v<I0, 1>(c, 0);
    }
    if constexpr (G % 3 == 1) issue_piece<base_of(T) + NRING + G / 3>(c, dep8(c.vf[ks & 1][1]));
  }
  if constexpr (T == 7 && G >= 37 && (G - 37) % 3 == 0) {
    constexpr int i = (G - 37) / 3;
    dma_dep_nt<i * 1024u, i * 32u>(c.ringM0, c.voffO, c.srdO, c.sO, dep8(c.vf[1][1]));
  }
  if constexpr (HAS_SM && T >= 1) {
    if constexpr (G == 0) {
#pragma unroll
      for (int qt = 0; qt < 3; ++qt) c.mxv[qt] = *(const LDS_AS f32x4*)(uintptr_t)(c.vMx + (unsigned)((T & 1) * 1536 + qt * 512));
    }
    if constexpr (G == 2) {
      bool need = false;
#pragma unroll
      for (int qt = 0; qt < 3; ++qt) { c.mxr[qt] = max2f(max3f(c.mxv[qt][0], c.mxv[qt][1], c.mxv[qt][2]), c.mxv[qt][3]); need |= (c.mxr[qt] - c.m[qt]) * SCL > THR; }
      c.resc = __any(need);
      if (__builtin_expect(c.resc, 0)) {
#pragma unroll
        for (int qt = 0; qt < 3; ++qt) { const float mn = fmaxf(c.m[qt], c.mxr[qt]); c.alpha[qt] = __builtin_amdgcn_exp2f((c.m[qt] - mn) * SCL); c.m[qt] = mn; c.l[qt] *= c.alpha[qt]; c.mc[qt] = -mn * SCL; }
      }
    }
  }
  if constexpr (HAS_SM) {
    constexpr int SM0 = 3, SM1 = (NG == 96) ? SM1_96 : 44;
    constexpr int e_lo = (G <= SM0) ? 0 : (G >= SM1) ? 48 : ((G - SM0) * 48) / (SM1 - SM0);
    constexpr int e_hi = (G + 1 <= SM0) ? 0 : (G + 1 >= SM1) ? 48 : ((G + 1 - SM0) * 48) / (SM1 - SM0);
    constexpr int p_lo = (G - 1 <= SM0) ? 0 : (G - 1 >= SM1) ? 48 : ((G - 1 - SM0) * 48) / (SM1 - SM0);
    constexpr int p_hi = e_lo;
    if constexpr (p_lo + 0 < p_hi) sm_add<X, p_lo + 0>(c);
    if constexpr (p_lo + 1 < p_hi) sm_add<X, p_lo + 1>(c);
    if constexpr (e_lo + 0 < e_hi) sm_exp<X, e_lo + 0>(c);
    if constexpr (e_lo + 1 < e_hi) sm_exp<X, e_lo + 1>(c);
    if constexpr ((p_hi >> 3) > (p_lo >> 3)) sm_pack<X, T & 1, (p_lo >> 3)>(c);
  }
  if constexpr (HAS_S && HAS_PV) {
    constexpr int M0 = MX0;
    if constexpr (G >= M0 && G < M0 + 24) { constexpr int i = G - M0; mx_step<XN, i / 8, i % 8>(c); }
    if constexpr (G >= M0 + 9 && G < M0 + 9 + 24 && ((G - M0 - 9) % 8) == 0) mx_fin<(T + 1) & 1, (G - M0 - 9) / 8>(c);
  }
  GAP();
}
template <int T, int... Gs> DI void gaps(Ctx& c, iseq<Gs...>) { (gap<T, Gs>(c), ...); }
template <int X, int PAR, int... Is> DI void mx_all(Ctx& c, iseq<Is...>) { (mx_step<X, Is / 8, Is % 8>(c), ...); mx_fin<PAR, 0>(c); mx_fin<PAR, 1>(c); mx_fin<PAR, 2>(c); }

template <int T> DI void interval(Ctx& c) {
  constexpr bool HAS_S = T < 7, HAS_PV = T >= 1;
  constexpr int NG = (HAS_S ? 48 : 0) + (HAS_PV ? 48 : 0);
  if constexpr (T >= 7) {
    rd_p<(T - 1) & 1, 0, 0>(c, 0); rd_p<(T - 1) & 1, 0, 1>(c, 0); rd_p<(T - 1) & 1, 0, 2>(c, 0);
  }
  c.resc = false;
  gaps<T>(c, make_iseq<NG>{});
  if constexpr (HAS_PV && T <= 7) {
    if (__builtin_expect(c.resc, 0)) {
      asm volatile("s_nop 7\n\ts_nop 7" : "+a"(c.o[0][0]), "+a"(c.o[0][1]), "+a"(c.o[0][2]), "+a"(c.o[1][0]), "+a"(c.o[1][1]), "+a"(c.o[1][2]));
#pragma unroll
      for (int dt = 0; dt < 2; ++dt)
#pragma unroll
        for (int qt = 0; qt < 3; ++qt)
#pragma unroll
          for (int r = 0; r < 16; ++r) c.o[dt][qt][r] *= c.alpha[qt];
      asm volatile("s_nop 1" : "+a"(c.o[0][0]), "+a"(c.o[0][1]), "+a"(c.o[0][2]), "+a"(c.o[1][0]), "+a"(c.o[1][1]), "+a"(c.o[1][2]));
    }
  }
  if constexpr (HAS_S && !HAS_PV) {
    asm volatile("s_nop 7\n\ts_nop 7" : "+v"(c.s[(T & 1) ^ 1][0]), "+v"(c.s[(T & 1) ^ 1][1]), "+v"(c.s[(T & 1) ^ 1][2]));
    mx_all<(T & 1) ^ 1, (T + 1) & 1>(c, make_iseq<24>{});
  }
  if constexpr (T < 8) barrier();
  GAP();
}

template <int Gr, int J> DI void qgap(Ctx& c) {
  constexpr int dt = J / 3, qt = J % 3;
  constexpr int ISS = NRING + 2 * Gr + (J > 1) + (J > 4);
  mfma_o<Gr == 0>(c.o[dt][qt], c.vf[Gr & 1][dt], c.pf[Gr & 1][qt]);
  if constexpr (Gr + 1 < 28) {
    constexpr int G1 = Gr + 1, cc1 = G1 >> 3, ks1 = G1 & 7;
    if constexpr (J == 0) { wait_gran<2 * G1, 2, ISS>(); rd_v<2 * G1, 0>(c, G1 & 1); }
    if constexpr (J == 2) rd_v<2 * G1, 1>(c, G1 & 1);
    if constexpr (ks1 != 0) {
      if constexpr (J == 3) { rd_p<cc1 & 1, ks1, 0>(c, G1 & 1); rd_p<cc1 & 1, ks1, 1>(c, G1 & 1); }
      if constexpr (J == 2) { rd_p<cc1 & 1, ks1, 2>(c, G1 & 1); }
    }
  } else {
    if constexpr (J == 0) { wait_gran<56, 4, ISS>(); rd_k<56, 0>(c, 0); }
    if constexpr (J == 2) rd_k<56, 1>(c, 0);
    if constexpr (J == 3) rd_k<56, 2>(c, 0);
    if constexpr (J == 5) rd_k<56, 3>(c, 0);
  }
  if constexpr (J == 1 || J == 4) issue_piece<2 * Gr + NRING + (J == 4)>(c, dep8(c.vf[Gr & 1][1]));
  GAP();
}
template <int Gr, int... Js> DI void qgran(Ctx& c, iseq<Js...>) { (qgap<Gr, Js>(c), ...); }

#define ST(i)
extern "C" __global__ __launch_bounds__(256, 1)
void attn_kernel(const unsigned short* __restrict__ x, const unsigned short* __restrict__ kc, const unsigned short* __restrict__ vc,
                 const unsigned short* __restrict__ wq, const unsigned short* __restrict__ wo, unsigned short* __restrict__ obuf,
                 unsigned short* __restrict__ wot, unsigned* __restrict__ cnt, unsigned long long* __restrict__ dbg) {
  extern __shared__ __attribute__((aligned(16))) char lds[];
  const unsigned ldsb = (unsigned)(uintptr_t)(LDS_AS char*)lds;
  const unsigned tid = threadIdx.x, lane = tid & 63u, l32 = lane & 31u, hi = lane >> 5;
  const unsigned w = __builtin_amdgcn_readfirstlane(tid >> 6);
  const unsigned bid = blockIdx.x, b = bid & 7u, idx = bid >> 3, h = idx >> 1, half = idx & 1u;
  const unsigned row0 = b * SQ + half * 96u;
  Ctx c;
  c.srdK = __builtin_amdgcn_make_buffer_rsrc((void*)(kc + (size_t)(b * NH + h) * SK * DH), (short)0, SK * DH * 2, 0x00020000);
  c.srdV = __builtin_amdgcn_make_buffer_rsrc((void*)(vc + (size_t)(b * NH + h) * SK * DH), (short)0, SK * DH * 2, 0x00020000);
  c.srdW = __builtin_amdgcn_make_buffer_rsrc((void*)wq, (short)0, DM * HD * 2, 0x00020000);
  c.srdX = __builtin_amdgcn_make_buffer_rsrc((void*)x, (short)0, NB * SQ * DM * 2, 0x00020000);
  {
    const unsigned a = lane >> 3, p7 = lane & 7u;
    c.voffK[0] = a * 512u + ((p7 ^ (a >> 1)) * 16u);
    c.voffK[1] = a * 512u + ((p7 ^ (a >> 1) ^ 4u) * 16u);
    c.voffV = ((a & 3u) + 8u * (a >> 2)) * 512u + ((p7 ^ ((a & 2u) << 1)) * 16u);
    c.voffW = a * 8192u + ((p7 ^ ((a & 2u) << 1)) * 16u);
    c.sK = w * 32u * 512u; c.sV = w * 128u; c.sW = h * 512u + w * 128u;
    c.ringM0 = ldsb + LDS_RING + w * (NRING * 1024u);
    c.vK = c.ringM0 + l32 * 128u + ((hi ^ ((l32 >> 1) & 7u)) * 16u);
    const unsigned g16 = lane >> 4, dblk = g16 & 1u, i16 = lane & 15u, q4 = i16 >> 2, p4 = i16 & 3u;
    c.vV = c.ringM0 + (8u * hi + q4) * 128u + (((2u * dblk + (p4 >> 1)) ^ ((q4 >> 1) << 2)) * 16u) + 8u * (p4 & 1u);
    c.vP = ldsb + LDS_P + l32 * 256u + ((hi ^ (l32 & 15u)) * 16u);
    c.vPw = ldsb + LDS_P + l32 * 256u + (((4u * w + hi) ^ (l32 & 15u)) * 16u);
    c.vMx = ldsb + LDS_MX + l32 * 16u; c.vMxw = c.vMx + w * 4u;
  }
  c.srdO = __builtin_amdgcn_make_buffer_rsrc((void*)wo, (short)0, HD * DM * 2, 0x00020000);
  c.voffO = (lane >> 1) * 896u + (lane & 1u) * 16u; c.sO = (bid >> 1) * 32u * 896u + (bid & 1u) * 448u + w * 128u;
  auto load_x = [&](int cc) {
#pragma unroll
    for (int i = 0; i < 6; ++i) {
      const unsigned pc = 6u * w + i;
      const unsigned voff = (lane >> 4) * 896u + ((((lane & 15u) ^ (lane >> 4)) ^ (4u * (pc & 3u))) * 16u);
      dma(ldsb + LDS_P + (unsigned)(cc & 1) * 24576u + pc * 1024u, voff, c.srdX, (row0 + 4u * pc) * 896u + (unsigned)cc * 256u);
    }
  };
  ST(0);
  if (bid == 0 && tid < 64) cnt[tid] = 0u;
  load_x(0);
  [&]<int... Is>(iseq<Is...>) { (issue_piece<Is>(c, 0u), ...); }(make_iseq<NRING>{});
  load_x(1);
  {
    const unsigned scr = ldsb + LDS_SCR + w * 256u;
    const unsigned g = (blockIdx.x * 4u + w) * 16u + (lane & 15u);
    const unsigned woff = (lane < 16u) ? (192u + (g >> 6)) * 8192u + (g & 63u) * 128u : 0xFFFFFF00u;
    asm volatile("s_mov_b32 m0, %0\n\ts_nop 0\n\tbuffer_load_dword %1, %2, 0 offen lds" :: "s"(scr), "v"(woff), "s"(c.srdW) : "memory");
    const unsigned xg = (blockIdx.x * 4u + w) * 5u + lane;
    const unsigned xr = xg / 3u, xc = xg - 3u * xr;
    const unsigned xoff = (lane < 5u && xg < 4608u) ? xr * 896u + 512u + xc * 128u : 0xFFFFFF00u;
    asm volatile("s_mov_b32 m0, %0\n\ts_nop 0\n\tbuffer_load_dword %1, %2, 0 offen lds" :: "s"(scr), "v"(xoff), "s"(c.srdX) : "memory");
  }
  wait_vm<NRING + 6 + 2>();
  barrier();
  ST(1);
  wait_gran<0, 2, NRING>(); rd_v<0, 0>(c, 0); rd_v<0, 1>(c, 0);
  rd_p<0, 0, 0>(c, 0); rd_p<0, 0, 1>(c, 0); rd_p<0, 0, 2>(c, 0);
  GAP();
  [&]<int... Gs>(iseq<Gs...>) {
    ([&] {
      qgran<Gs>(c, make_iseq<6>{});
      if constexpr ((Gs & 7) == 7) {
        constexpr int cc1 = (Gs + 1) >> 3;
        wait_vm<14>();
        barrier();
        rd_p<cc1 & 1, 0, 0>(c, (Gs + 1) & 1); rd_p<cc1 & 1, 0, 1>(c, (Gs + 1) & 1); rd_p<cc1 & 1, 0, 2>(c, (Gs + 1) & 1);
        if constexpr (cc1 + 1 < 4) load_x(cc1 + 1);
        GAP();
      }
    }(), ...);
  }(make_iseq<28>{});
  barrier();
  ST(2);
  asm volatile("s_nop 7\n\ts_nop 7" : "+a"(c.o[0][0]), "+a"(c.o[0][1]), "+a"(c.o[0][2]), "+a"(c.o[1][0]), "+a"(c.o[1][1]), "+a"(c.o[1][2]));
#pragma unroll
  for (int qt = 0; qt < 3; ++qt)
#pragma unroll
    for (int dt = 0; dt < 2; ++dt)
#pragma unroll
      for (int g = 0; g < 4; ++g) {
        u32x2 v = {cvtpk(c.o[dt][qt][4 * g], c.o[dt][qt][4 * g + 1]), cvtpk(c.o[dt][qt][4 * g + 2], c.o[dt][qt][4 * g + 3])};
        const unsigned ci = 8u * w + 4u * dt + g;
        *(LDS_AS u32x2*)(uintptr_t)(ldsb + LDS_P + (32u * qt + l32) * 512u + ((ci ^ (l32 & 15u)) * 16u) + 8u * hi) = v;
      }
  barrier();
#pragma unroll
  for (int qt = 0; qt < 3; ++qt)
#pragma unroll
    for (int ks = 0; ks < 16; ++ks)
      c.q[qt * 16 + ks] = lds_b128(ldsb + LDS_P + (32u * qt + l32) * 512u + (((2u * ks + hi) ^ (l32 & 15u)) * 16u));
  GAP();
  ST(3);
  interval<-1>(c);
  ST(4);
  {
#pragma unroll
    for (int qt = 0; qt < 3; ++qt) {
      f32x4 v = *(const LDS_AS f32x4*)(uintptr_t)(c.vMx + (unsigned)(qt * 512));
      c.m[qt] = fmaxf(fmaxf(v[0], v[1]), fmaxf(v[2], v[3])); c.l[qt] = 0.f; c.mc[qt] = -c.m[qt] * SCL;
    }
  }
  GAP();
  interval<0>(c); ST(5); interval<1>(c); ST(6); interval<2>(c); ST(7); interval<3>(c); ST(8); interval<4>(c); ST(9);
  interval<5>(c); ST(10); interval<6>(c); ST(11); interval<7>(c); ST(12);
  interval<8>(c); ST(13);
  {
    wait_vm<0>();
    {
      const unsigned g16 = lane >> 4, i16 = lane & 15u, q4 = i16 >> 2, p4 = i16 & 3u;
#pragma unroll
      for (int i = 0; i < 4; ++i) {
        const unsigned nt = 4u * w + i;
        const unsigned a = c.ringM0 + i * 1024u + (8u * g16 + q4) * 32u + p4 * 8u;
        bf16x4 lo = lds_tr(a), hi4 = lds_tr(a + 4u * 32u);
        bf16x8 v = __builtin_shufflevector(lo, hi4, 0, 1, 2, 3, 4, 5, 6, 7);
        if (nt < 14u) *(bf16x8*)(wot + ((size_t)((bid >> 1) * 28u + (bid & 1u) * 14u + nt) * 64u + lane) * 8u) = v;
      }
    }
#pragma unroll
    for (int qt = 0; qt < 3; ++qt) {
      auto pr = __builtin_amdgcn_permlane32_swap(__float_as_uint(c.l[qt]), __float_as_uint(c.l[qt]), false, false);
      const float lt = __uint_as_float(pr[0]) + __uint_as_float(pr[1]);
      *(LDS_AS float*)(uintptr_t)(c.vMxw + (unsigned)(qt * 512)) = lt;
    }
    barrier();
    asm volatile("s_nop 7\n\ts_nop 7" : "+a"(c.o[0][0]), "+a"(c.o[0][1]), "+a"(c.o[0][2]), "+a"(c.o[1][0]), "+a"(c.o[1][1]), "+a"(c.o[1][2]));
#pragma unroll
    for (int qt = 0; qt < 3; ++qt) {
      f32x4 v = *(const LDS_AS f32x4*)(uintptr_t)(c.vMx + (unsigned)(qt * 512));
      const float inv = 1.0f / ((v[0] + v[1]) + (v[2] + v[3]));
#pragma unroll
      for (int dt = 0; dt < 2; ++dt)
#pragma unroll
        for (int g = 0; g < 4; ++g) {
          u32x2 o2 = {cvtpk(c.o[dt][qt][4 * g] * inv, c.o[dt][qt][4 * g + 1] * inv), cvtpk(c.o[dt][qt][4 * g + 2] * inv, c.o[dt][qt][4 * g + 3] * inv)};
          const unsigned ci = 4u * dt + g;
          *(LDS_AS u32x2*)(uintptr_t)(c.ringM0 + (32u * qt + l32) * 128u + ((ci ^ (l32 & 7u)) * 16u) + 8u * hi) = o2;
        }
    }
    asm volatile("s_waitcnt lgkmcnt(0)" ::: "memory");
#pragma unroll
    for (int i = 0; i < 12; ++i) {
      const unsigned mt = i >> 1, ksl = i & 1, q = 16u * mt + (lane & 15u), ci = 4u * ksl + (lane >> 4);
      u32x4 v = *(const LDS_AS u32x4*)(uintptr_t)(c.ringM0 + q * 128u + ((ci ^ (q & 7u)) * 16u));
      *(u32x4*)(obuf + ((size_t)((row0 / 16u + mt) * 128u + 8u * h + 2u * w + ksl) * 64u + lane) * 8u) = v;
    }
  }
  ST(14);
}

#define MFMA16(a, b, c) __builtin_amdgcn_mfma_f32_16x16x32_bf16((a), (b), (c), 0, 0, 0)
constexpr int OPD = 6;
extern "C" __global__ __launch_bounds__(512, 2)
void oproj_kernel(const unsigned short* __restrict__ obuf, const unsigned short* __restrict__ wot, unsigned short* __restrict__ out) {
  extern __shared__ __attribute__((aligned(16))) char lds[];
  const unsigned tid = threadIdx.x, lane = tid & 63u;
  const unsigned w = __builtin_amdgcn_readfirstlane(tid >> 6);
  const unsigned bid = blockIdx.x, xcd = bid & 7u, j = bid >> 3;
  const unsigned ng = j % 7u, mg = xcd * 4u + j / 7u;
  const unsigned ks0 = w * 16u;
  const bf16x8* A = (const bf16x8*)obuf + ((size_t)(mg * 3u) * 128u + ks0) * 64u + lane;
  const bf16x8* Bm = (const bf16x8*)wot + ((size_t)ks0 * 28u + ng * 4u) * 64u + lane;
  f32x4 acc[3][4];
#pragma unroll
  for (int m = 0; m < 3; ++m)
#pragma unroll
    for (int n = 0; n < 4; ++n) acc[m][n] = f32x4{0.f, 0.f, 0.f, 0.f};
  bf16x8 af[OPD][3], bfr[OPD][4];
#pragma unroll
  for (int p = 0; p < OPD; ++p) {
#pragma unroll
    for (int m = 0; m < 3; ++m) af[p][m] = A[(m * 128 + p) * 64];
#pragma unroll
    for (int n = 0; n < 4; ++n) bfr[p][n] = Bm[(p * 28 + n) * 64];
  }
  GAP();
#pragma unroll
  for (int ks = 0; ks < 16; ++ks) {
#pragma unroll
    for (int m = 0; m < 3; ++m)
#pragma unroll
      for (int n = 0; n < 4; ++n) acc[m][n] = MFMA16(af[ks % OPD][m], bfr[ks % OPD][n], acc[m][n]);
    GAP();
    if (ks + OPD < 16) {
#pragma unroll
      for (int m = 0; m < 3; ++m) af[ks % OPD][m] = A[(m * 128 + ks + OPD) * 64];
#pragma unroll
      for (int n = 0; n < 4; ++n) bfr[ks % OPD][n] = Bm[((ks + OPD) * 28 + n) * 64];
    }
    GAP();
  }
  f32x4* L = (f32x4*)lds;
#pragma unroll
  for (int m = 0; m < 3; ++m)
#pragma unroll
    for (int n = 0; n < 4; ++n) L[(w * 12u + m * 4 + n) * 64u + lane] = acc[m][n];
  __syncthreads();
  for (unsigned it = tid; it < 768u; it += 512u) {
    f32x4 sum = L[it];
#pragma unroll
    for (int ww = 1; ww < 8; ++ww) sum += L[ww * 768u + it];
    const unsigned t = it >> 6, l = it & 63u, m = t >> 2, n = t & 3u;
    const unsigned row = mg * 48u + 16u * m + 4u * (l >> 4), col = ng * 64u + 16u * n + (l & 15u);
    const unsigned p01 = cvtpk(sum[0], sum[1]), p23 = cvtpk(sum[2], sum[3]);
    out[(size_t)(row + 0) * DM + col] = (unsigned short)(p01 & 0xFFFFu);
    out[(size_t)(row + 1) * DM + col] = (unsigned short)(p01 >> 16);
    out[(size_t)(row + 2) * DM + col] = (unsigned short)(p23 & 0xFFFFu);
    out[(size_t)(row + 3) * DM + col] = (unsigned short)(p23 >> 16);
  }
}

extern "C" void kernel_launch(void* const* d_in, const int* in_sizes, int n_in, void* d_out, int out_size, void* d_ws, size_t ws_size, hipStream_t stream) {
  const unsigned short* x = (const unsigned short*)d_in[0]; const unsigned short* kc = (const unsigned short*)d_in[1];
  const unsigned short* vc = (const unsigned short*)d_in[2]; const unsigned short* wq = (const unsigned short*)d_in[3];
  const unsigned short* wo = (const unsigned short*)d_in[4];
  char* ws = (char*)d_ws;
  unsigned short* obuf = (unsigned short*)ws;
  unsigned short* wot = (unsigned short*)(ws + (16u << 20));
  float* slab = (float*)(ws + (24u << 20));
  unsigned* cnt = (unsigned*)(ws + (40u << 20));
  (void)hipFuncSetAttribute((const void*)attn_kernel, hipFuncAttributeMaxDynamicSharedMemorySize, (int)LDS_TOTAL);
  (void)hipFuncSetAttribute((const void*)oproj_kernel, hipFuncAttributeMaxDynamicSharedMemorySize, 8 * 12 * 1024);
  attn_kernel<<<256, 256, LDS_TOTAL, stream>>>(x, kc, vc, wq, wo, obuf, wot, cnt, (unsigned long long*)(ws + (64u << 20)));
  oproj_kernel<<<224, 512, 8 * 12 * 1024, stream>>>(obuf, wot, (unsigned short*)d_out);
}
```

```cpp
#include <hip/hip_runtime.h>
#include <hip/hip_bf16.h>
#include <stdint.h>
#include <utility>

typedef __bf16 bf16x8 __attribute__((ext_vector_type(8)));
typedef __bf16 bf16x4 __attribute__((ext_vector_type(4)));
typedef __bf16 bf16x2 __attribute__((ext_vector_type(2)));
typedef float f32x16 __attribute__((ext_vector_type(16)));
typedef float f32x4 __attribute__((ext_vector_type(4)));
typedef float f32x2 __attribute__((ext_vector_type(2)));
typedef unsigned u32x4 __attribute__((ext_vector_type(4)));
typedef unsigned u32x2 __attribute__((ext_vector_type(2)));
#define DI __device__ __forceinline__
#define LDS_AS __attribute__((address_space(3)))
#define GAP() __builtin_amdgcn_sched_barrier(0)
template <int... Is> using iseq = std::integer_sequence<int, Is...>;
template <int N> using make_iseq = std::make_integer_sequence<int, N>;

constexpr int NB = 8, SQ = 192, DM = 448, NH = 16, SK = 1024, DH = 256, HD = NH * DH;
constexpr int NRING = 24;
constexpr int NPIECE = 376;
constexpr unsigned LDS_RING = 0, LDS_P = 4 * NRING * 1024, LDS_MX = LDS_P + 49152, LDS_SCR = LDS_MX + 3072, LDS_TOTAL = 163840;
constexpr float SCL = 0.0625f * 1.4426950408889634f;
constexpr float THR = 8.0f;
#define SM1_96 60
#define MX0 58
constexpr int NQA = 40;

struct Piece { int type, tile, p; };
constexpr Piece piece_of(int idx) {
  if (idx < 56) return Piece{0, idx >> 1, idx & 1};
  int j = idx - 56;
  if (j < 32) return Piece{1, j >> 4, j & 15};
  j -= 32;
  if (j < 192) { int blk = j >> 5, r = j & 31; return r < 16 ? Piece{1, blk + 2, r} : Piece{2, blk, r - 16}; }
  j -= 192;
  if (j < 32) return Piece{2, 6 + (j >> 4), j & 15};
  j -= 32;
  if (j < 64) return Piece{4, j >> 1, j & 1};
  return Piece{3, 0, 0};
}
constexpr int imin(int a, int b) { return a < b ? a : b; }

struct Ctx {
  __amdgpu_buffer_rsrc_t srdK, srdV, srdW, srdX, srdO;
  unsigned voffO, sO;
  unsigned voffK[2], voffV, voffW;
  unsigned sK, sV, sW;
  unsigned ringM0;
  unsigned vK, vV, vP, vPw, vMx, vMxw;
  bf16x8 q[48];
  f32x16 o[2][3];
  f32x16 s[2][3];
  bf16x8 kf[2][4], vf[2][2], pf[2][3];
  float m[3], l[3], mc[3], alpha[3], mxr[3]; f32x4 mxv[3]; bool resc;
  unsigned short* part;
};

DI unsigned cvtpk(float lo, float hi) { f32x2 v = {lo, hi}; bf16x2 b = __builtin_convertvector(v, bf16x2); return __builtin_bit_cast(unsigned, b); }
DI float max3f(float a, float b, float c) { float r; asm("v_max3_f32 %0, %1, %2, %3" : "=v"(r) : "v"(a), "v"(b), "v"(c)); return r; }
DI float max2f(float a, float b) { float r; asm("v_max_f32_e32 %0, %1, %2" : "=v"(r) : "v"(a), "v"(b)); return r; }
DI float fadd_s(float a, float b) { float r; asm("v_add_f32_e32 %0, %1, %2" : "=v"(r) : "v"(a), "v"(b)); return r; }
template <int N> DI void wait_vm() { asm volatile("s_waitcnt vmcnt(%0)" :: "i"(N) : "memory"); }
DI void barrier() { asm volatile("s_waitcnt lgkmcnt(0)\n\ts_barrier" ::: "memory"); }
DI void dma(unsigned m0, unsigned voff, __amdgpu_buffer_rsrc_t srd, unsigned soff) {
  asm volatile("s_mov_b32 m0, %0\n\ts_nop 0\n\tbuffer_load_dwordx4 %1, %2, %3 offen lds" :: "s"(m0), "v"(voff), "s"(srd), "s"(soff) : "memory");
}
DI void dma_dep(unsigned m0, unsigned voff, __amdgpu_buffer_rsrc_t srd, unsigned soff, unsigned dep) {
  asm volatile("s_mov_b32 m0, %0\n\ts_nop 0\n\tbuffer_load_dwordx4 %1, %2, %3 offen lds" :: "s"(m0), "v"(voff), "s"(srd), "s"(soff), "v"(dep) : "memory");
}
template <unsigned M0OFF, unsigned SOFF> DI void dma_dep_nt(unsigned m0b, unsigned voff, __amdgpu_buffer_rsrc_t srd, unsigned sb, unsigned dep) {
  unsigned tmp;
  asm volatile("s_add_u32 m0, %1, %6\n\ts_add_u32 %0, %4, %7\n\tbuffer_load_dwordx4 %2, %3, %0 offen nt lds" : "=&s"(tmp) : "s"(m0b), "v"(voff), "s"(srd), "s"(sb), "v"(dep), "i"(M0OFF), "i"(SOFF) : "memory", "scc");
}
template <int IDX> DI void issue_piece(const Ctx& c, unsigned dep) {
  constexpr Piece pc = piece_of(IDX);
  if constexpr (pc.type != 3) {
    const unsigned m0 = c.ringM0 + (unsigned)(IDX % NRING) * 1024u;
    if constexpr (pc.type == 0) dma_dep(m0, c.voffW, c.srdW, c.sW + (unsigned)(16 * pc.tile + 8 * pc.p) * 8192u, dep);
    else if constexpr (pc.type == 1) dma_dep_nt<(unsigned)(IDX % NRING) * 1024u, (unsigned)(128 * pc.tile + 8 * (pc.p & 3)) * 512u + (unsigned)(pc.p >> 2) * 128u>(c.ringM0, c.voffK[pc.p & 1], c.srdK, c.sK, dep);
    else if constexpr (pc.type == 2) dma_dep_nt<(unsigned)(IDX % NRING) * 1024u, (unsigned)(128 * pc.tile + 16 * (pc.p >> 1) + 4 * (pc.p & 1)) * 512u>(c.ringM0, c.voffV, c.srdV, c.sV, dep);
    else dma_dep(m0, c.voffO, c.srdO, c.sO + (unsigned)(pc.tile >> 4) * 512u + (unsigned)(16 * (pc.tile & 15) + 8 * pc.p) * 896u, dep);
  }
}
DI bf16x8 lds_b128(unsigned addr) { return *(const LDS_AS bf16x8*)(uintptr_t)addr; }
DI bf16x4 lds_tr(unsigned addr) { return __builtin_amdgcn_ds_read_tr16_b64_v4bf16((LDS_AS bf16x4*)(uintptr_t)addr); }
DI unsigned dep8(const bf16x8& v) { return __builtin_bit_cast(u32x4, v)[3]; }

template <int QI, bool ZERO> DI void mfma_s(f32x16& s, const bf16x8& kf, const bf16x8& qf) {
  if constexpr (QI < NQA) {
    if constexpr (ZERO) asm volatile("v_mfma_f32_32x32x16_bf16 %0, %1, %2, 0" : "=&v"(s) : "v"(kf), "a"(qf));
    else                asm volatile("v_mfma_f32_32x32x16_bf16 %0, %1, %2, %0" : "+v"(s) : "v"(kf), "a"(qf));
  } else {
    if constexpr (ZERO) asm volatile("v_mfma_f32_32x32x16_bf16 %0, %1, %2, 0" : "=&v"(s) : "v"(kf), "v"(qf));
    else                asm volatile("v_mfma_f32_32x32x16_bf16 %0, %1, %2, %0" : "+v"(s) : "v"(kf), "v"(qf));
  }
}
template <bool ZERO> DI void mfma_o(f32x16& o, const bf16x8& vf, const bf16x8& pf) {
  if constexpr (ZERO) asm volatile("v_mfma_f32_32x32x16_bf16 %0, %1, %2, 0" : "=&a"(o) : "v"(vf), "v"(pf));
  else                asm volatile("v_mfma_f32_32x32x16_bf16 %0, %1, %2, %0" : "+a"(o) : "v"(vf), "v"(pf));
}

template <bool ZERO> DI void mfma_v(f32x16& o, const bf16x8& vf, const bf16x8& pf) {
  if constexpr (ZERO) asm volatile("v_mfma_f32_32x32x16_bf16 %0, %1, %2, 0" : "=&v"(o) : "v"(vf), "v"(pf));
  else                asm volatile("v_mfma_f32_32x32x16_bf16 %0, %1, %2, %0" : "+v"(o) : "v"(vf), "v"(pf));
}

template <int I0, int S> DI void rd_k(Ctx& c, int buf) { c.kf[buf][S] = lds_b128((c.vK ^ (32u * S)) + (unsigned)(I0 % NRING) * 1024u); }
template <int I0, int DT> DI void rd_v(Ctx& c, int buf) {
  bf16x4 lo = lds_tr((c.vV ^ (64u * DT)) + (unsigned)(I0 % NRING) * 1024u);
  bf16x4 hi = lds_tr((c.vV ^ (64u * DT)) + (unsigned)(I0 % NRING) * 1024u + 512u);
  c.vf[buf][DT] = __builtin_shufflevector(lo, hi, 0, 1, 2, 3, 4, 5, 6, 7);
}
template <int BUF, int KS, int QT> DI void rd_p(Ctx& c, int buf) { c.pf[buf][QT] = lds_b128((c.vP ^ (32u * KS)) + (unsigned)(QT * 8192 + BUF * 24576)); }
constexpr int NEXTRA = 8;
template <int I0, int G, int ISSUED, int YOUNGER = 0> DI void wait_gran() {
  static_assert(imin(NPIECE, ISSUED) >= I0 + G, "not issued yet");
  constexpr int n = imin(NPIECE, ISSUED) - (I0 + G) + ((I0 + G <= NRING) ? NEXTRA : 0) + YOUNGER;
  wait_vm<(n > 63 ? 63 : n)>();
}
constexpr int base_of(int T) { return (T == -1) ? 56 : (T == 0) ? 72 : (T <= 6) ? 88 + 32 * (T - 1) : 280 + 16 * (T - 7); }
constexpr int issued_at(int T, int G) { return NRING + base_of(T) + (G + 1) / 3; }

constexpr int ik_of(int T) { return (T == -1) ? 56 : (T == 0) ? 72 : 88 + 32 * (T - 1); }
constexpr int iv_of(int T) { return (T <= 6) ? 88 + 32 * (T - 1) + 16 : 280 + 16 * (T - 7); }

template <int X, int E> DI void sm_exp(Ctx& c) { c.s[X][E >> 4][E & 15] = __builtin_amdgcn_exp2f(fmaf(c.s[X][E >> 4][E & 15], SCL, c.mc[E >> 4])); }
template <int X, int E> DI void sm_add(Ctx& c) { c.l[E >> 4] = fadd_s(c.l[E >> 4], c.s[X][E >> 4][E & 15]); }
template <int X, int BUF, int CH> DI void sm_pack(Ctx& c) {
  constexpr int qt = CH >> 1, ss = CH & 1;
  const f32x16& p = c.s[X][qt];
  u32x4 v = {cvtpk(p[8 * ss], p[8 * ss + 1]), cvtpk(p[8 * ss + 2], p[8 * ss + 3]), cvtpk(p[8 * ss + 4], p[8 * ss + 5]), cvtpk(p[8 * ss + 6], p[8 * ss + 7])};
  *(LDS_AS u32x4*)(uintptr_t)((c.vPw ^ (32u * ss)) + (unsigned)(qt * 8192 + BUF * 24576)) = v;
}
template <int X, int QT, int I> DI void mx_step(Ctx& c) {
  if constexpr (I == 0) c.mxr[QT] = max2f(c.s[X][QT][0], c.s[X][QT][1]);
  else c.mxr[QT] = max3f(c.mxr[QT], c.s[X][QT][2 * I], c.s[X][QT][2 * I + 1]);
}
template <int PAR, int QT> DI void mx_fin(Ctx& c) {
  auto pr = __builtin_amdgcn_permlane32_swap(__float_as_uint(c.mxr[QT]), __float_as_uint(c.mxr[QT]), false, false);
  const float mx = max2f(__uint_as_float(pr[0]), __uint_as_float(pr[1]));
  *(LDS_AS float*)(uintptr_t)(c.vMxw + (unsigned)(PAR * 1536 + QT * 512)) = mx;
}

template <int T, int G> DI void gap(Ctx& c) {
  constexpr bool HAS_S = T < 7, HAS_SM = T >= 0 && T < 8, HAS_PV = T >= 1;
  constexpr int NS = HAS_S ? 48 : 0, NG = NS + (HAS_PV ? 48 : 0);
  constexpr int X = T & 1, XN = X ^ 1;
  constexpr int IK = ik_of(T), IV = iv_of(T);
  if constexpr (G < NS) {
    constexpr int g = G / 12, s = (G % 12) / 3, qt = G % 3;
    mfma_s<qt * 16 + 4 * g + s, (g == 0 && s == 0)>(c.s[XN][qt], c.kf[g & 1][s], c.q[qt * 16 + 4 * g + s]);
  } else {
    constexpr int P = G - NS, ks = P / 6, dt = (P % 6) / 3, qt = P % 3;
    mfma_o<(T == 1 && ks == 0)>(c.o[dt][qt], c.vf[ks & 1][dt], c.pf[ks & 1][qt]);
  }
  constexpr int ISS = issued_at(T, G);
  if constexpr (G < NS) {
    constexpr int g = G / 12, j = G % 12;
    if constexpr (g < 3) {
      constexpr int I0 = IK + 4 * (g + 1);
      if constexpr (j == 0) { wait_gran<I0, 4, ISS>(); rd_k<I0, 0>(c, (g + 1) & 1); }
      if constexpr (j == 2) rd_k<I0, 1>(c, (g + 1) & 1);
      if constexpr (j == 3) rd_k<I0, 2>(c, (g + 1) & 1);
      if constexpr (j == 5) rd_k<I0, 3>(c, (g + 1) & 1);
    } else if constexpr (HAS_PV) {
      if constexpr (j == 0) { wait_gran<IV, 2, ISS>(); rd_v<IV, 0>(c, 0); }
      if constexpr (j == 2) rd_v<IV, 1>(c, 0);
      if constexpr (j == 3) rd_p<(T - 1) & 1, 0, 0>(c, 0);
      if constexpr (j == 5) rd_p<(T - 1) & 1, 0, 1>(c, 0);
      if constexpr (j == 6) rd_p<(T - 1) & 1, 0, 2>(c, 0);
    } else {
      constexpr int I0 = ik_of(T + 1);
      if constexpr (j == 0) { wait_gran<I0, 4, ISS>(); rd_k<I0, 0>(c, 0); }
      if constexpr (j == 2) rd_k<I0, 1>(c, 0);
      if constexpr (j == 3) rd_k<I0, 2>(c, 0);
      if constexpr (j == 5) rd_k<I0, 3>(c, 0);
    }
    if constexpr (G % 3 == 1) issue_piece<base_of(T) + NRING + G / 3>(c, dep8(c.kf[g & 1][3]));
  } else {
    constexpr int P = G - NS, ks = P / 6, j = P % 6;
    if constexpr (ks < 7) {
      constexpr int I0 = IV + 2 * (ks + 1);
      if constexpr (j == 0) { wait_gran<I0, 2, ISS>(); rd_v<I0, 0>(c, (ks + 1) & 1); }
      if constexpr (j == 2) { rd_v<I0, 1>(c, (ks + 1) & 1); }
      if constexpr (j == 3) { rd_p<(T - 1) & 1, ks + 1, 0>(c, (ks + 1) & 1); rd_p<(T - 1) & 1, ks + 1, 1>(c, (ks + 1) & 1); }
      if constexpr (j == 4) { rd_p<(T - 1) & 1, ks + 1, 2>(c, (ks + 1) & 1); }
    } else if constexpr (T < 6) {
      constexpr int I0 = ik_of(T + 1);
      if constexpr (j == 0) { wait_gran<I0, 4, ISS>(); rd_k<I0, 0>(c, 0); }
      if constexpr (j == 2) rd_k<I0, 1>(c, 0);
      if constexpr (j == 3) rd_k<I0, 2>(c, 0);
      if constexpr (j == 5) rd_k<I0, 3>(c, 0);
    } else {
      constexpr int I0 = (T < 8) ? iv_of(T + 1) : 312;
      if constexpr (j == 0) { wait_gran<I0, 2, ISS>(); rd_v<I0, 0>(c, 0); }
      if constexpr (j == 2) rd_v<I0, 1>(c, 0);
    }
    if constexpr (G % 3 == 1) issue_piece<base_of(T) + NRING + G / 3>(c, dep8(c.vf[ks & 1][1]));
  }
  if constexpr (T == 8 && G == 1) {
#pragma unroll
    for (int qt = 0; qt < 3; ++qt) c.mxv[qt] = *(const LDS_AS f32x4*)(uintptr_t)(c.vMx + (unsigned)(qt * 512));
  }
  if constexpr (T == 8 && G == 4) {
#pragma unroll
    for (int qt = 0; qt < 3; ++qt) c.alpha[qt] = 1.0f / ((c.mxv[qt][0] + c.mxv[qt][1]) + (c.mxv[qt][2] + c.mxv[qt][3]));
  }
  if constexpr (HAS_SM && T >= 1) {
    if constexpr (G == 0) {
#pragma unroll
      for (int qt = 0; qt < 3; ++qt) c.mxv[qt] = *(const LDS_AS f32x4*)(uintptr_t)(c.vMx + (unsigned)((T & 1) * 1536 + qt * 512));
    }
    if constexpr (G == 2) {
      bool need = false;
#pragma unroll
      for (int qt = 0; qt < 3; ++qt) { c.mxr[qt] = max2f(max3f(c.mxv[qt][0], c.mxv[qt][1], c.mxv[qt][2]), c.mxv[qt][3]); need |= (c.mxr[qt] - c.m[qt]) * SCL > THR; }
      c.resc = __any(need);
      if (__builtin_expect(c.resc, 0)) {
#pragma unroll
        for (int qt = 0; qt < 3; ++qt) { const float mn = fmaxf(c.m[qt], c.mxr[qt]); c.alpha[qt] = __builtin_amdgcn_exp2f((c.m[qt] - mn) * SCL); c.m[qt] = mn; c.l[qt] *= c.alpha[qt]; c.mc[qt] = -mn * SCL; }
      }
    }
  }
  if constexpr (HAS_SM) {
    constexpr int SM0 = 3, SM1 = (NG == 96) ? SM1_96 : 44;
    constexpr int e_lo = (G <= SM0) ? 0 : (G >= SM1) ? 48 : ((G - SM0) * 48) / (SM1 - SM0);
    constexpr int e_hi = (G + 1 <= SM0) ? 0 : (G + 1 >= SM1) ? 48 : ((G + 1 - SM0) * 48) / (SM1 - SM0);
    constexpr int p_lo = (G - 1 <= SM0) ? 0 : (G - 1 >= SM1) ? 48 : ((G - 1 - SM0) * 48) / (SM1 - SM0);
    constexpr int p_hi = e_lo;
    if constexpr (p_lo + 0 < p_hi) sm_add<X, p_lo + 0>(c);
    if constexpr (p_lo + 1 < p_hi) sm_add<X, p_lo + 1>(c);
    if constexpr (e_lo + 0 < e_hi) sm_exp<X, e_lo + 0>(c);
    if constexpr (e_lo + 1 < e_hi) sm_exp<X, e_lo + 1>(c);
    if constexpr ((p_hi >> 3) > (p_lo >> 3)) sm_pack<X, T & 1, (p_lo >> 3)>(c);
  }
  if constexpr (HAS_S && HAS_PV) {
    constexpr int M0 = MX0;
    if constexpr (G >= M0 && G < M0 + 24) { constexpr int i = G - M0; mx_step<XN, i / 8, i % 8>(c); }
    if constexpr (G >= M0 + 9 && G < M0 + 9 + 24 && ((G - M0 - 9) % 8) == 0) mx_fin<(T + 1) & 1, (G - M0 - 9) / 8>(c);
  }
  GAP();
}
template <int T, int... Gs> DI void gaps(Ctx& c, iseq<Gs...>) { (gap<T, Gs>(c), ...); }
template <int X, int PAR, int... Is> DI void mx_all(Ctx& c, iseq<Is...>) { (mx_step<X, Is / 8, Is % 8>(c), ...); mx_fin<PAR, 0>(c); mx_fin<PAR, 1>(c); mx_fin<PAR, 2>(c); }

template <int T> DI void interval(Ctx& c) {
  constexpr bool HAS_S = T < 7, HAS_PV = T >= 1;
  constexpr int NG = (HAS_S ? 48 : 0) + (HAS_PV ? 48 : 0);
  if constexpr (T >= 7) {
    rd_p<(T - 1) & 1, 0, 0>(c, 0); rd_p<(T - 1) & 1, 0, 1>(c, 0); rd_p<(T - 1) & 1, 0, 2>(c, 0);
  }
  c.resc = false;
  gaps<T>(c, make_iseq<NG>{});
  if constexpr (HAS_PV && T <= 7) {
    if (__builtin_expect(c.resc, 0)) {
      asm volatile("s_nop 7\n\ts_nop 7" : "+a"(c.o[0][0]), "+a"(c.o[0][1]), "+a"(c.o[0][2]), "+a"(c.o[1][0]), "+a"(c.o[1][1]), "+a"(c.o[1][2]));
#pragma unroll
      for (int dt = 0; dt < 2; ++dt)
#pragma unroll
        for (int qt = 0; qt < 3; ++qt)
#pragma unroll
          for (int r = 0; r < 16; ++r) c.o[dt][qt][r] *= c.alpha[qt];
      asm volatile("s_nop 1" : "+a"(c.o[0][0]), "+a"(c.o[0][1]), "+a"(c.o[0][2]), "+a"(c.o[1][0]), "+a"(c.o[1][1]), "+a"(c.o[1][2]));
    }
  }
  if constexpr (HAS_S && !HAS_PV) {
    asm volatile("s_nop 7\n\ts_nop 7" : "+v"(c.s[(T & 1) ^ 1][0]), "+v"(c.s[(T & 1) ^ 1][1]), "+v"(c.s[(T & 1) ^ 1][2]));
    mx_all<(T & 1) ^ 1, (T + 1) & 1>(c, make_iseq<24>{});
  }
  if constexpr (T == 7) {
#pragma unroll
    for (int qt = 0; qt < 3; ++qt) {
      auto pr = __builtin_amdgcn_permlane32_swap(__float_as_uint(c.l[qt]), __float_as_uint(c.l[qt]), false, false);
      *(LDS_AS float*)(uintptr_t)(c.vMxw + (unsigned)(qt * 512)) = __uint_as_float(pr[0]) + __uint_as_float(pr[1]);
    }
  }
  barrier();
  GAP();
}

template <int Gr, int J> DI void qgap(Ctx& c) {
  constexpr int dt = J / 3, qt = J % 3;
  constexpr int ISS = NRING + 2 * Gr + (J > 1) + (J > 4);
  mfma_o<Gr == 0>(c.o[dt][qt], c.vf[Gr & 1][dt], c.pf[Gr & 1][qt]);
  if constexpr (Gr + 1 < 28) {
    constexpr int G1 = Gr + 1, cc1 = G1 >> 3, ks1 = G1 & 7;
    if constexpr (J == 0) { wait_gran<2 * G1, 2, ISS>(); rd_v<2 * G1, 0>(c, G1 & 1); }
    if constexpr (J == 2) rd_v<2 * G1, 1>(c, G1 & 1);
    if constexpr (ks1 != 0) {
      if constexpr (J == 3) { rd_p<cc1 & 1, ks1, 0>(c, G1 & 1); rd_p<cc1 & 1, ks1, 1>(c, G1 & 1); }
      if constexpr (J == 2) { rd_p<cc1 & 1, ks1, 2>(c, G1 & 1); }
    }
  } else {
    if constexpr (J == 0) { wait_gran<56, 4, ISS>(); rd_k<56, 0>(c, 0); }
    if constexpr (J == 2) rd_k<56, 1>(c, 0);
    if constexpr (J == 3) rd_k<56, 2>(c, 0);
    if constexpr (J == 5) rd_k<56, 3>(c, 0);
  }
  if constexpr (J == 1 || J == 4) issue_piece<2 * Gr + NRING + (J == 4)>(c, dep8(c.vf[Gr & 1][1]));
  GAP();
}
template <int Gr, int... Js> DI void qgran(Ctx& c, iseq<Js...>) { (qgap<Gr, Js>(c), ...); }

template <int I> DI void store_part_a(Ctx& c) {
  constexpr int t = I >> 1, j = I & 1, dt = t / 3, qt = t % 3;
  asm volatile("" : "+a"(c.o[dt][qt]));
  const f32x16& a = c.o[dt][qt];
  u32x4 v = {cvtpk(a[8 * j], a[8 * j + 1]), cvtpk(a[8 * j + 2], a[8 * j + 3]), cvtpk(a[8 * j + 4], a[8 * j + 5]), cvtpk(a[8 * j + 6], a[8 * j + 7])};
  *(u32x4*)(c.part + I * 512) = v;
}
template <int I> DI void store_part_b(Ctx& c) {
  constexpr int t = I >> 1, j = I & 1, dt = t / 3, qt = t % 3;
  const f32x16& a = c.s[dt][qt];
  u32x4 v = {cvtpk(a[8 * j], a[8 * j + 1]), cvtpk(a[8 * j + 2], a[8 * j + 3]), cvtpk(a[8 * j + 4], a[8 * j + 5]), cvtpk(a[8 * j + 6], a[8 * j + 7])};
  *(u32x4*)(c.part + (48 + I) * 512) = v;
}
template <int PASS, int Gr, int J> DI void ogap(Ctx& c) {
  constexpr int dt = J / 3, qt = J % 3, IB = 312 + 32 * PASS, GG = 16 * PASS + Gr;
  constexpr int ISS = NRING + IB + 2 * Gr + (J > 1) + (J > 4);
  constexpr int YS = (PASS == 1 && Gr > 4) ? Gr - 4 : 0;
  if constexpr (PASS == 0) mfma_o<Gr == 0>(c.o[dt][qt], c.vf[GG & 1][dt], c.pf[GG & 1][qt]);
  else mfma_v<Gr == 0>(c.s[dt][qt], c.vf[GG & 1][dt], c.pf[GG & 1][qt]);
  if constexpr (GG + 1 < 32) {
    constexpr int G1 = GG + 1, k1 = G1 & 15;
    if constexpr (J == 0) { wait_gran<312 + 2 * G1, 2, ISS, YS>(); rd_v<312 + 2 * G1, 0>(c, G1 & 1); }
    if constexpr (J == 2) { rd_v<312 + 2 * G1, 1>(c, G1 & 1); rd_p<(k1 >> 3), k1 & 7, 2>(c, G1 & 1); }
    if constexpr (J == 3) { rd_p<(k1 >> 3), k1 & 7, 0>(c, G1 & 1); rd_p<(k1 >> 3), k1 & 7, 1>(c, G1 & 1); }
  }
  if constexpr (J == 1 || J == 4) issue_piece<IB + 2 * Gr + NRING + (J == 4)>(c, dep8(c.vf[GG & 1][1]));
  if constexpr (PASS == 1 && Gr >= 4 && J == 5) store_part_a<Gr - 4>(c);
  GAP();
}
template <int PASS, int Gr, int... Js> DI void ogran(Ctx& c, iseq<Js...>) { (ogap<PASS, Gr, Js>(c), ...); }
template <int PASS, int... Gs> DI void opass(Ctx& c, iseq<Gs...>) { (ogran<PASS, Gs>(c, make_iseq<6>{}), ...); }
template <int... Is> DI void store_all_a(Ctx& c, iseq<Is...>) { (store_part_a<Is>(c), ...); }
template <int... Is> DI void store_all_b(Ctx& c, iseq<Is...>) { (store_part_b<Is>(c), ...); }

#define ST(i)
extern "C" __global__ __launch_bounds__(256, 1)
void attn_kernel(const unsigned short* __restrict__ x, const unsigned short* __restrict__ kc, const unsigned short* __restrict__ vc,
                 const unsigned short* __restrict__ wq, const unsigned short* __restrict__ wo, unsigned short* __restrict__ part,
                 unsigned long long* __restrict__ dbg) {
  extern __shared__ __attribute__((aligned(16))) char lds[];
  const unsigned ldsb = (unsigned)(uintptr_t)(LDS_AS char*)lds;
  const unsigned tid = threadIdx.x, lane = tid & 63u, l32 = lane & 31u, hi = lane >> 5;
  const unsigned w = __builtin_amdgcn_readfirstlane(tid >> 6);
  const unsigned bid = blockIdx.x, b = bid & 7u, idx = bid >> 3, h = idx >> 1, half = idx & 1u;
  const unsigned row0 = b * SQ + half * 96u;
  Ctx c;
  c.srdK = __builtin_amdgcn_make_buffer_rsrc((void*)(kc + (size_t)(b * NH + h) * SK * DH), (short)0, SK * DH * 2, 0x00020000);
  c.srdV = __builtin_amdgcn_make_buffer_rsrc((void*)(vc + (size_t)(b * NH + h) * SK * DH), (short)0, SK * DH * 2, 0x00020000);
  c.srdW = __builtin_amdgcn_make_buffer_rsrc((void*)wq, (short)0, DM * HD * 2, 0x00020000);
  c.srdX = __builtin_amdgcn_make_buffer_rsrc((void*)x, (short)0, NB * SQ * DM * 2, 0x00020000);
  {
    const unsigned a = lane >> 3, p7 = lane & 7u;
    c.voffK[0] = a * 512u + ((p7 ^ (a >> 1)) * 16u);
    c.voffK[1] = a * 512u + ((p7 ^ (a >> 1) ^ 4u) * 16u);
    c.voffV = ((a & 3u) + 8u * (a >> 2)) * 512u + ((p7 ^ ((a & 2u) << 1)) * 16u);
    c.voffW = a * 8192u + ((p7 ^ ((a & 2u) << 1)) * 16u);
    c.sK = w * 32u * 512u; c.sV = w * 128u; c.sW = h * 512u + w * 128u;
    c.ringM0 = ldsb + LDS_RING + w * (NRING * 1024u);
    c.vK = c.ringM0 + l32 * 128u + ((hi ^ ((l32 >> 1) & 7u)) * 16u);
    const unsigned g16 = lane >> 4, dblk = g16 & 1u, i16 = lane & 15u, q4 = i16 >> 2, p4 = i16 & 3u;
    c.vV = c.ringM0 + (8u * hi + q4) * 128u + (((2u * dblk + (p4 >> 1)) ^ ((q4 >> 1) << 2)) * 16u) + 8u * (p4 & 1u);
    c.vP = ldsb + LDS_P + l32 * 256u + ((hi ^ (l32 & 15u)) * 16u);
    c.vPw = ldsb + LDS_P + l32 * 256u + (((4u * w + hi) ^ (l32 & 15u)) * 16u);
    c.vMx = ldsb + LDS_MX + l32 * 16u; c.vMxw = c.vMx + w * 4u;
  }
  c.srdO = __builtin_amdgcn_make_buffer_rsrc((void*)wo, (short)0, HD * DM * 2, 0x00020000);
  c.voffO = (lane >> 3) * 896u + (((lane & 7u) ^ (((lane >> 3) & 2u) << 1)) * 16u); c.sO = h * 256u * 896u + w * 128u;
  c.part = part + ((size_t)(((b * NH + h) * 2u + half) * 7u + w) * 12u) * 512u + lane * 8u;
  auto load_x = [&](int cc) {
#pragma unroll
    for (int i = 0; i < 6; ++i) {
      const unsigned pc = 6u * w + i;
      const unsigned voff = (lane >> 4) * 896u + ((((lane & 15u) ^ (lane >> 4)) ^ (4u * (pc & 3u))) * 16u);
      dma(ldsb + LDS_P + (unsigned)(cc & 1) * 24576u + pc * 1024u, voff, c.srdX, (row0 + 4u * pc) * 896u + (unsigned)cc * 256u);
    }
  };
  ST(0);
  load_x(0);
  [&]<int... Is>(iseq<Is...>) { (issue_piece<Is>(c, 0u), ...); }(make_iseq<NRING>{});
  load_x(1);
  {
    const unsigned scr = ldsb + LDS_SCR + w * 256u;
    const unsigned g = (blockIdx.x * 4u + w) * 16u + (lane & 15u);
    const unsigned woff = (lane < 16u) ? (192u + (g >> 6)) * 8192u + (g & 63u) * 128u : 0xFFFFFF00u;
    asm volatile("s_mov_b32 m0, %0\n\ts_nop 0\n\tbuffer_load_dword %1, %2, 0 offen lds" :: "s"(scr), "v"(woff), "s"(c.srdW) : "memory");
    const unsigned xg = (blockIdx.x * 4u + w) * 5u + lane;
    const unsigned xr = xg / 3u, xc = xg - 3u * xr;
    const unsigned xoff = (lane < 5u && xg < 4608u) ? xr * 896u + 512u + xc * 128u : 0xFFFFFF00u;
    asm volatile("s_mov_b32 m0, %0\n\ts_nop 0\n\tbuffer_load_dword %1, %2, 0 offen lds" :: "s"(scr), "v"(xoff), "s"(c.srdX) : "memory");
  }
  wait_vm<NRING + 6 + 2>();
  barrier();
  ST(1);
  wait_gran<0, 2, NRING>(); rd_v<0, 0>(c, 0); rd_v<0, 1>(c, 0);
  rd_p<0, 0, 0>(c, 0); rd_p<0, 0, 1>(c, 0); rd_p<0, 0, 2>(c, 0);
  GAP();
  [&]<int... Gs>(iseq<Gs...>) {
    ([&] {
      qgran<Gs>(c, make_iseq<6>{});
      if constexpr ((Gs & 7) == 7) {
        constexpr int cc1 = (Gs + 1) >> 3;
        wait_vm<14>();
        barrier();
        rd_p<cc1 & 1, 0, 0>(c, (Gs + 1) & 1); rd_p<cc1 & 1, 0, 1>(c, (Gs + 1) & 1); rd_p<cc1 & 1, 0, 2>(c, (Gs + 1) & 1);
        if constexpr (cc1 + 1 < 4) load_x(cc1 + 1);
        GAP();
      }
    }(), ...);
  }(make_iseq<28>{});
  barrier();
  ST(2);
  asm volatile("s_nop 7\n\ts_nop 7" : "+a"(c.o[0][0]), "+a"(c.o[0][1]), "+a"(c.o[0][2]), "+a"(c.o[1][0]), "+a"(c.o[1][1]), "+a"(c.o[1][2]));
#pragma unroll
  for (int qt = 0; qt < 3; ++qt)
#pragma unroll
    for (int dt = 0; dt < 2; ++dt)
#pragma unroll
      for (int g = 0; g < 4; ++g) {
        u32x2 v = {cvtpk(c.o[dt][qt][4 * g], c.o[dt][qt][4 * g + 1]), cvtpk(c.o[dt][qt][4 * g + 2], c.o[dt][qt][4 * g + 3])};
        const unsigned ci = 8u * w + 4u * dt + g;
        *(LDS_AS u32x2*)(uintptr_t)(ldsb + LDS_P + (32u * qt + l32) * 512u + ((ci ^ (l32 & 15u)) * 16u) + 8u * hi) = v;
      }
  barrier();
#pragma unroll
  for (int qt = 0; qt < 3; ++qt)
#pragma unroll
    for (int ks = 0; ks < 16; ++ks)
      c.q[qt * 16 + ks] = lds_b128(ldsb + LDS_P + (32u * qt + l32) * 512u + (((2u * ks + hi) ^ (l32 & 15u)) * 16u));
  GAP();
  ST(3);
  interval<-1>(c);
  ST(4);
  {
#pragma unroll
    for (int qt = 0; qt < 3; ++qt) {
      f32x4 v = *(const LDS_AS f32x4*)(uintptr_t)(c.vMx + (unsigned)(qt * 512));
      c.m[qt] = fmaxf(fmaxf(v[0], v[1]), fmaxf(v[2], v[3])); c.l[qt] = 0.f; c.mc[qt] = -c.m[qt] * SCL;
    }
  }
  GAP();
  interval<0>(c); ST(5); interval<1>(c); ST(6); interval<2>(c); ST(7); interval<3>(c); ST(8); interval<4>(c); ST(9);
  interval<5>(c); ST(10); interval<6>(c); ST(11); interval<7>(c); ST(12);
  interval<8>(c); ST(13);
  asm volatile("s_nop 7\n\ts_nop 7" : "+a"(c.o[0][0]), "+a"(c.o[0][1]), "+a"(c.o[0][2]), "+a"(c.o[1][0]), "+a"(c.o[1][1]), "+a"(c.o[1][2]));
#pragma unroll
  for (int qt = 0; qt < 3; ++qt) {
    const float inv = c.alpha[qt];
#pragma unroll
    for (int dt = 0; dt < 2; ++dt)
#pragma unroll
      for (int g = 0; g < 4; ++g) {
        u32x2 o2 = {cvtpk(c.o[dt][qt][4 * g] * inv, c.o[dt][qt][4 * g + 1] * inv), cvtpk(c.o[dt][qt][4 * g + 2] * inv, c.o[dt][qt][4 * g + 3] * inv)};
        const unsigned ci = 8u * (w & 1u) + 4u * dt + g;
        *(LDS_AS u32x2*)(uintptr_t)(ldsb + LDS_P + (w >> 1) * 24576u + (32u * qt + l32) * 256u + ((ci ^ (l32 & 15u)) * 16u) + 8u * hi) = o2;
      }
  }
  barrier();
  rd_p<0, 0, 0>(c, 0); rd_p<0, 0, 1>(c, 0); rd_p<0, 0, 2>(c, 0);
  GAP();
  opass<0>(c, make_iseq<16>{});
  asm volatile("s_nop 7\n\ts_nop 7" : "+a"(c.o[0][0]), "+a"(c.o[0][1]), "+a"(c.o[0][2]), "+a"(c.o[1][0]), "+a"(c.o[1][1]), "+a"(c.o[1][2]));
  if (w < 3u) {
    opass<1>(c, make_iseq<16>{});
    asm volatile("s_nop 7\n\ts_nop 7" : "+v"(c.s[0][0]), "+v"(c.s[0][1]), "+v"(c.s[0][2]), "+v"(c.s[1][0]), "+v"(c.s[1][1]), "+v"(c.s[1][2]));
    store_all_b(c, make_iseq<12>{});
  } else {
    asm volatile("s_nop 7\n\ts_nop 7" : "+a"(c.o[0][0]), "+a"(c.o[0][1]), "+a"(c.o[0][2]), "+a"(c.o[1][0]), "+a"(c.o[1][1]), "+a"(c.o[1][2]));
    store_all_a(c, make_iseq<12>{});
  }
  ST(14);
}

extern "C" __global__ __launch_bounds__(256)
void reduce_kernel(const unsigned short* __restrict__ part, unsigned short* __restrict__ out) {
  const unsigned idx = blockIdx.x * 256u + threadIdx.x, lane = idx & 63u, rest = idx >> 6;
  const unsigned blk = rest % 84u, bh2 = rest / 84u, half = bh2 & 1u, b = bh2 >> 1;
  const u32x4* p = (const u32x4*)(part + ((size_t)((b * NH) * 2u + half) * 84u + blk) * 512u + lane * 8u);
  u32x4 v[16];
#pragma unroll
  for (int h = 0; h < 16; ++h) v[h] = p[(size_t)h * (2u * 84u * 64u)];
  float acc[8] = {0.f, 0.f, 0.f, 0.f, 0.f, 0.f, 0.f, 0.f};
#pragma unroll
  for (int h = 0; h < 16; ++h)
#pragma unroll
    for (int e = 0; e < 4; ++e) { acc[2 * e] += __uint_as_float(v[h][e] << 16); acc[2 * e + 1] += __uint_as_float(v[h][e] & 0xFFFF0000u); }
  const unsigned slice = blk / 12u, tj = blk - 12u * slice, tile = tj >> 1, j = tj & 1u, dt = tile / 3u, qt = tile - 3u * dt;
  const unsigned row = b * SQ + half * 96u + 32u * qt + (lane & 31u), n0 = 64u * slice + 32u * dt + 16u * j + 4u * (lane >> 5);
  u32x2 o0 = {cvtpk(acc[0], acc[1]), cvtpk(acc[2], acc[3])}, o1 = {cvtpk(acc[4], acc[5]), cvtpk(acc[6], acc[7])};
  *(u32x2*)(out + (size_t)row * DM + n0) = o0;
  *(u32x2*)(out + (size_t)row * DM + n0 + 8u) = o1;
}

extern "C" void kernel_launch(void* const* d_in, const int* in_sizes, int n_in, void* d_out, int out_size, void* d_ws, size_t ws_size, hipStream_t stream) {
  const unsigned short* x = (const unsigned short*)d_in[0]; const unsigned short* kc = (const unsigned short*)d_in[1];
  const unsigned short* vc = (const unsigned short*)d_in[2]; const unsigned short* wq = (const unsigned short*)d_in[3];
  const unsigned short* wo = (const unsigned short*)d_in[4];
  char* ws = (char*)d_ws;
  unsigned short* part = (unsigned short*)ws;
  (void)hipFuncSetAttribute((const void*)attn_kernel, hipFuncAttributeMaxDynamicSharedMemorySize, (int)LDS_TOTAL);
  attn_kernel<<<256, 256, LDS_TOTAL, stream>>>(x, kc, vc, wq, wo, part, (unsigned long long*)(ws + (64u << 20)));
  reduce_kernel<<<336, 256, 0, stream>>>(part, (unsigned short*)d_out);
}
```

```cpp
#include <hip/hip_runtime.h>
#include <hip/hip_bf16.h>
#include <stdint.h>
#include <utility>

typedef __bf16 bf16x8 __attribute__((ext_vector_type(8)));
typedef __bf16 bf16x4 __attribute__((ext_vector_type(4)));
typedef __bf16 bf16x2 __attribute__((ext_vector_type(2)));
typedef float f32x16 __attribute__((ext_vector_type(16)));
typedef float f32x4 __attribute__((ext_vector_type(4)));
typedef float f32x2 __attribute__((ext_vector_type(2)));
typedef unsigned u32x4 __attribute__((ext_vector_type(4)));
typedef unsigned u32x2 __attribute__((ext_vector_type(2)));
#define DI __device__ __forceinline__
#define LDS_AS __attribute__((address_space(3)))
#define GAP() __builtin_amdgcn_sched_barrier(0)
template <int... Is> using iseq = std::integer_sequence<int, Is...>;
template <int N> using make_iseq = std::make_integer_sequence<int, N>;

constexpr int NB = 8, SQ = 192, DM = 448, NH = 16, SK = 1024, DH = 256, HD = NH * DH;
constexpr int NRING = 24;
constexpr int NPIECE = 376;
constexpr unsigned LDS_RING = 0, LDS_P = 4 * NRING * 1024, LDS_MX = LDS_P + 49152, LDS_SCR = LDS_MX + 3072, LDS_TOTAL = 163840;
constexpr float SCL = 0.0625f * 1.4426950408889634f;
constexpr float THR = 8.0f;
#define SM1_96 60
#define MX0 58
#define PSTORE_AUX 16
constexpr int NQA = 40;

struct Piece { int type, tile, p; };
constexpr Piece piece_of(int idx) {
  if (idx < 56) return Piece{0, idx >> 1, idx & 1};
  int j = idx - 56;
  if (j < 32) return Piece{1, j >> 4, j & 15};
  j -= 32;
  if (j < 192) { int blk = j >> 5, r = j & 31; return r < 16 ? Piece{1, blk + 2, r} : Piece{2, blk, r - 16}; }
  j -= 192;
  if (j < 32) return Piece{2, 6 + (j >> 4), j & 15};
  j -= 32;
  if (j < 64) return Piece{4, j >> 1, j & 1};
  return Piece{3, 0, 0};
}
constexpr int imin(int a, int b) { return a < b ? a : b; }

struct Ctx {
  __amdgpu_buffer_rsrc_t srdK, srdV, srdW, srdX, srdO;
  unsigned voffO, sO;
  unsigned voffK[2], voffV, voffW;
  unsigned sK, sV, sW;
  unsigned ringM0;
  unsigned vK, vV, vP, vPw, vMx, vMxw;
  bf16x8 q[48];
  f32x16 o[2][3];
  f32x16 s[2][3];
  bf16x8 kf[2][4], vf[2][2], pf[2][3];
  float m[3], l[3], mc[3], alpha[3], mxr[3]; f32x4 mxv[3]; bool resc;
  __amdgpu_buffer_rsrc_t srdP; unsigned vpart;
};

DI unsigned cvtpk(float lo, float hi) { f32x2 v = {lo, hi}; bf16x2 b = __builtin_convertvector(v, bf16x2); return __builtin_bit_cast(unsigned, b); }
DI float max3f(float a, float b, float c) { float r; asm("v_max3_f32 %0, %1, %2, %3" : "=v"(r) : "v"(a), "v"(b), "v"(c)); return r; }
DI float max2f(float a, float b) { float r; asm("v_max_f32_e32 %0, %1, %2" : "=v"(r) : "v"(a), "v"(b)); return r; }
DI float fadd_s(float a, float b) { float r; asm("v_add_f32_e32 %0, %1, %2" : "=v"(r) : "v"(a), "v"(b)); return r; }
template <int N> DI void wait_vm() { asm volatile("s_waitcnt vmcnt(%0)" :: "i"(N) : "memory"); }
DI void barrier() { asm volatile("s_waitcnt lgkmcnt(0)\n\ts_barrier" ::: "memory"); }
DI void dma(unsigned m0, unsigned voff, __amdgpu_buffer_rsrc_t srd, unsigned soff) {
  asm volatile("s_mov_b32 m0, %0\n\ts_nop 0\n\tbuffer_load_dwordx4 %1, %2, %3 offen lds" :: "s"(m0), "v"(voff), "s"(srd), "s"(soff) : "memory");
}
DI void dma_dep(unsigned m0, unsigned voff, __amdgpu_buffer_rsrc_t srd, unsigned soff, unsigned dep) {
  asm volatile("s_mov_b32 m0, %0\n\ts_nop 0\n\tbuffer_load_dwordx4 %1, %2, %3 offen lds" :: "s"(m0), "v"(voff), "s"(srd), "s"(soff), "v"(dep) : "memory");
}
template <unsigned M0OFF, unsigned SOFF> DI void dma_dep_nt(unsigned m0b, unsigned voff, __amdgpu_buffer_rsrc_t srd, unsigned sb, unsigned dep) {
  unsigned tmp;
  asm volatile("s_add_u32 m0, %1, %6\n\ts_add_u32 %0, %4, %7\n\tbuffer_load_dwordx4 %2, %3, %0 offen nt lds" : "=&s"(tmp) : "s"(m0b), "v"(voff), "s"(srd), "s"(sb), "v"(dep), "i"(M0OFF), "i"(SOFF) : "memory", "scc");
}
template <int IDX> DI void issue_piece(const Ctx& c, unsigned dep) {
  constexpr Piece pc = piece_of(IDX);
  if constexpr (pc.type != 3) {
    const unsigned m0 = c.ringM0 + (unsigned)(IDX % NRING) * 1024u;
    if constexpr (pc.type == 0) dma_dep(m0, c.voffW, c.srdW, c.sW + (unsigned)(16 * pc.tile + 8 * pc.p) * 8192u, dep);
    else if constexpr (pc.type == 1) dma_dep_nt<(unsigned)(IDX % NRING) * 1024u, (unsigned)(128 * pc.tile + 8 * (pc.p & 3)) * 512u + (unsigned)(pc.p >> 2) * 128u>(c.ringM0, c.voffK[pc.p & 1], c.srdK, c.sK, dep);
    else if constexpr (pc.type == 2) dma_dep_nt<(unsigned)(IDX % NRING) * 1024u, (unsigned)(128 * pc.tile + 16 * (pc.p >> 1) + 4 * (pc.p & 1)) * 512u>(c.ringM0, c.voffV, c.srdV, c.sV, dep);
    else dma_dep(m0, c.voffO, c.srdO, c.sO + (unsigned)(pc.tile >> 4) * 512u + (unsigned)(16 * (pc.tile & 15) + 8 * pc.p) * 896u, dep);
  }
}
DI bf16x8 lds_b128(unsigned addr) { return *(const LDS_AS bf16x8*)(uintptr_t)addr; }
DI bf16x4 lds_tr(unsigned addr) { return __builtin_amdgcn_ds_read_tr16_b64_v4bf16((LDS_AS bf16x4*)(uintptr_t)addr); }
DI unsigned dep8(const bf16x8& v) { return __builtin_bit_cast(u32x4, v)[3]; }

template <int QI, bool ZERO> DI void mfma_s(f32x16& s, const bf16x8& kf, const bf16x8& qf) {
  if constexpr (QI < NQA) {
    if constexpr (ZERO) asm volatile("v_mfma_f32_32x32x16_bf16 %0, %1, %2, 0" : "=&v"(s) : "v"(kf), "a"(qf));
    else                asm volatile("v_mfma_f32_32x32x16_bf16 %0, %1, %2, %0" : "+v"(s) : "v"(kf), "a"(qf));
  } else {
    if constexpr (ZERO) asm volatile("v_mfma_f32_32x32x16_bf16 %0, %1, %2, 0" : "=&v"(s) : "v"(kf), "v"(qf));
    else                asm volatile("v_mfma_f32_32x32x16_bf16 %0, %1, %2, %0" : "+v"(s) : "v"(kf), "v"(qf));
  }
}
template <bool ZERO> DI void mfma_o(f32x16& o, const bf16x8& vf, const bf16x8& pf) {
  if constexpr (ZERO) asm volatile("v_mfma_f32_32x32x16_bf16 %0, %1, %2, 0" : "=&a"(o) : "v"(vf), "v"(pf));
  else                asm volatile("v_mfma_f32_32x32x16_bf16 %0, %1, %2, %0" : "+a"(o) : "v"(vf), "v"(pf));
}

template <bool ZERO> DI void mfma_v(f32x16& o, const bf16x8& vf, const bf16x8& pf) {
  if constexpr (ZERO) asm volatile("v_mfma_f32_32x32x16_bf16 %0, %1, %2, 0" : "=&v"(o) : "v"(vf), "v"(pf));
  else                asm volatile("v_mfma_f32_32x32x16_bf16 %0, %1, %2, %0" : "+v"(o) : "v"(vf), "v"(pf));
}

template <int I0, int S> DI void rd_k(Ctx& c, int buf) { c.kf[buf][S] = lds_b128((c.vK ^ (32u * S)) + (unsigned)(I0 % NRING) * 1024u); }
template <int I0, int DT> DI void rd_v(Ctx& c, int buf) {
  bf16x4 lo = lds_tr((c.vV ^ (64u * DT)) + (unsigned)(I0 % NRING) * 1024u);
  bf16x4 hi = lds_tr((c.vV ^ (64u * DT)) + (unsigned)(I0 % NRING) * 1024u + 512u);
  c.vf[buf][DT] = __builtin_shufflevector(lo, hi, 0, 1, 2, 3, 4, 5, 6, 7);
}
template <int BUF, int KS, int QT> DI void rd_p(Ctx& c, int buf) { c.pf[buf][QT] = lds_b128((c.vP ^ (32u * KS)) + (unsigned)(QT * 8192 + BUF * 24576)); }
constexpr int NEXTRA = 8;
template <int I0, int G, int ISSUED, int YOUNGER = 0> DI void wait_gran() {
  static_assert(imin(NPIECE, ISSUED) >= I0 + G, "not issued yet");
  constexpr int n = imin(NPIECE, ISSUED) - (I0 + G) + ((I0 + G <= NRING) ? NEXTRA : 0) + YOUNGER;
  wait_vm<(n > 63 ? 63 : n)>();
}
constexpr int base_of(int T) { return (T == -1) ? 56 : (T == 0) ? 72 : (T <= 6) ? 88 + 32 * (T - 1) : 280 + 16 * (T - 7); }
constexpr int issued_at(int T, int G) { return NRING + base_of(T) + (G + 1) / 3; }

constexpr int ik_of(int T) { return (T == -1) ? 56 : (T == 0) ? 72 : 88 + 32 * (T - 1); }
constexpr int iv_of(int T) { return (T <= 6) ? 88 + 32 * (T - 1) + 16 : 280 + 16 * (T - 7); }

template <int X, int E> DI void sm_exp(Ctx& c) { c.s[X][E >> 4][E & 15] = __builtin_amdgcn_exp2f(fmaf(c.s[X][E >> 4][E & 15], SCL, c.mc[E >> 4])); }
template <int X, int E> DI void sm_add(Ctx& c) { c.l[E >> 4] = fadd_s(c.l[E >> 4], c.s[X][E >> 4][E & 15]); }
template <int X, int BUF, int CH> DI void sm_pack(Ctx& c) {
  constexpr int qt = CH >> 1, ss = CH & 1;
  const f32x16& p = c.s[X][qt];
  u32x4 v = {cvtpk(p[8 * ss], p[8 * ss + 1]), cvtpk(p[8 * ss + 2], p[8 * ss + 3]), cvtpk(p[8 * ss + 4], p[8 * ss + 5]), cvtpk(p[8 * ss + 6], p[8 * ss + 7])};
  *(LDS_AS u32x4*)(uintptr_t)((c.vPw ^ (32u * ss)) + (unsigned)(qt * 8192 + BUF * 24576)) = v;
}
template <int X, int QT, int I> DI void mx_step(Ctx& c) {
  if constexpr (I == 0) c.mxr[QT] = max2f(c.s[X][QT][0], c.s[X][QT][1]);
  else c.mxr[QT] = max3f(c.mxr[QT], c.s[X][QT][2 * I], c.s[X][QT][2 * I + 1]);
}
template <int PAR, int QT> DI void mx_fin(Ctx& c) {
  auto pr = __builtin_amdgcn_permlane32_swap(__float_as_uint(c.mxr[QT]), __float_as_uint(c.mxr[QT]), false, false);
  const float mx = max2f(__uint_as_float(pr[0]), __uint_as_float(pr[1]));
  *(LDS_AS float*)(uintptr_t)(c.vMxw + (unsigned)(PAR * 1536 + QT * 512)) = mx;
}

template <int T, int G> DI void gap(Ctx& c) {
  constexpr bool HAS_S = T < 7, HAS_SM = T >= 0 && T < 8, HAS_PV = T >= 1;
  constexpr int NS = HAS_S ? 48 : 0, NG = NS + (HAS_PV ? 48 : 0);
  constexpr int X = T & 1, XN = X ^ 1;
  constexpr int IK = ik_of(T), IV = iv_of(T);
  if constexpr (G < NS) {
    constexpr int g = G / 12, s = (G % 12) / 3, qt = G % 3;
    mfma_s<qt * 16 + 4 * g + s, (g == 0 && s == 0)>(c.s[XN][qt], c.kf[g & 1][s], c.q[qt * 16 + 4 * g + s]);
  } else {
    constexpr int P = G - NS, ks = P / 6, dt = (P % 6) / 3, qt = P % 3;
    mfma_o<(T == 1 && ks == 0)>(c.o[dt][qt], c.vf[ks & 1][dt], c.pf[ks & 1][qt]);
  }
  constexpr int ISS = issued_at(T, G);
  if constexpr (G < NS) {
    constexpr int g = G / 12, j = G % 12;
    if constexpr (g < 3) {
      constexpr int I0 = IK + 4 * (g + 1);
      if constexpr (j == 0) { wait_gran<I0, 4, ISS>(); rd_k<I0, 0>(c, (g + 1) & 1); }
      if constexpr (j == 2) rd_k<I0, 1>(c, (g + 1) & 1);
      if constexpr (j == 3) rd_k<I0, 2>(c, (g + 1) & 1);
      if constexpr (j == 5) rd_k<I0, 3>(c, (g + 1) & 1);
    } else if constexpr (HAS_PV) {
      if constexpr (j == 0) { wait_gran<IV, 2, ISS>(); rd_v<IV, 0>(c, 0); }
      if constexpr (j == 2) rd_v<IV, 1>(c, 0);
      if constexpr (j == 3) rd_p<(T - 1) & 1, 0, 0>(c, 0);
      if constexpr (j == 5) rd_p<(T - 1) & 1, 0, 1>(c, 0);
      if constexpr (j == 6) rd_p<(T - 1) & 1, 0, 2>(c, 0);
    } else {
      constexpr int I0 = ik_of(T + 1);
      if constexpr (j == 0) { wait_gran<I0, 4, ISS>(); rd_k<I0, 0>(c, 0); }
      if constexpr (j == 2) rd_k<I0, 1>(c, 0);
      if constexpr (j == 3) rd_k<I0, 2>(c, 0);
      if constexpr (j == 5) rd_k<I0, 3>(c, 0);
    }
    if constexpr (G % 3 == 1) issue_piece<base_of(T) + NRING + G / 3>(c, dep8(c.kf[g & 1][3]));
  } else {
    constexpr int P = G - NS, ks = P / 6, j = P % 6;
    if constexpr (ks < 7) {
      constexpr int I0 = IV + 2 * (ks + 1);
      if constexpr (j == 0) { wait_gran<I0, 2, ISS>(); rd_v<I0, 0>(c, (ks + 1) & 1); }
      if constexpr (j == 2) { rd_v<I0, 1>(c, (ks + 1) & 1); }
      if constexpr (j == 3) { rd_p<(T - 1) & 1, ks + 1, 0>(c, (ks + 1) & 1); rd_p<(T - 1) & 1, ks + 1, 1>(c, (ks + 1) & 1); }
      if constexpr (j == 4) { rd_p<(T - 1) & 1, ks + 1, 2>(c, (ks + 1) & 1); }
    } else if constexpr (T < 6) {
      constexpr int I0 = ik_of(T + 1);
      if constexpr (j == 0) { wait_gran<I0, 4, ISS>(); rd_k<I0, 0>(c, 0); }
      if constexpr (j == 2) rd_k<I0, 1>(c, 0);
      if constexpr (j == 3) rd_k<I0, 2>(c, 0);
      if constexpr (j == 5) rd_k<I0, 3>(c, 0);
    } else {
      constexpr int I0 = (T < 8) ? iv_of(T + 1) : 312;
      if constexpr (j == 0) { wait_gran<I0, 2, ISS>(); rd_v<I0, 0>(c, 0); }
      if constexpr (j == 2) rd_v<I0, 1>(c, 0);
    }
    if constexpr (G % 3 == 1) issue_piece<base_of(T) + NRING + G / 3>(c, dep8(c.vf[ks & 1][1]));
  }
  if constexpr (T == 8 && G == 1) {
#pragma unroll
    for (int qt = 0; qt < 3; ++qt) c.mxv[qt] = *(const LDS_AS f32x4*)(uintptr_t)(c.vMx + (unsigned)(qt * 512));
  }
  if constexpr (T == 8 && G == 4) {
#pragma unroll
    for (int qt = 0; qt < 3; ++qt) c.alpha[qt] = 1.0f / ((c.mxv[qt][0] + c.mxv[qt][1]) + (c.mxv[qt][2] + c.mxv[qt][3]));
  }
  if constexpr (HAS_SM && T >= 1) {
    if constexpr (G == 0) {
#pragma unroll
      for (int qt = 0; qt < 3; ++qt) c.mxv[qt] = *(const LDS_AS f32x4*)(uintptr_t)(c.vMx + (unsigned)((T & 1) * 1536 + qt * 512));
    }
    if constexpr (G == 2) {
      bool need = false;
#pragma unroll
      for (int qt = 0; qt < 3; ++qt) { c.mxr[qt] = max2f(max3f(c.mxv[qt][0], c.mxv[qt][1], c.mxv[qt][2]), c.mxv[qt][3]); need |= (c.mxr[qt] - c.m[qt]) * SCL > THR; }
      c.resc = __any(need);
      if (__builtin_expect(c.resc, 0)) {
#pragma unroll
        for (int qt = 0; qt < 3; ++qt) { const float mn = fmaxf(c.m[qt], c.mxr[qt]); c.alpha[qt] = __builtin_amdgcn_exp2f((c.m[qt] - mn) * SCL); c.m[qt] = mn; c.l[qt] *= c.alpha[qt]; c.mc[qt] = -mn * SCL; }
      }
    }
  }
  if constexpr (HAS_SM) {
    constexpr int SM0 = 3, SM1 = (NG == 96) ? SM1_96 : 44;
    constexpr int e_lo = (G <= SM0) ? 0 : (G >= SM1) ? 48 : ((G - SM0) * 48) / (SM1 - SM0);
    constexpr int e_hi = (G + 1 <= SM0) ? 0 : (G + 1 >= SM1) ? 48 : ((G + 1 - SM0) * 48) / (SM1 - SM0);
    constexpr int p_lo = (G - 1 <= SM0) ? 0 : (G - 1 >= SM1) ? 48 : ((G - 1 - SM0) * 48) / (SM1 - SM0);
    constexpr int p_hi = e_lo;
    if constexpr (p_lo + 0 < p_hi) sm_add<X, p_lo + 0>(c);
    if constexpr (p_lo + 1 < p_hi) sm_add<X, p_lo + 1>(c);
    if constexpr (e_lo + 0 < e_hi) sm_exp<X, e_lo + 0>(c);
    if constexpr (e_lo + 1 < e_hi) sm_exp<X, e_lo + 1>(c);
    if constexpr ((p_hi >> 3) > (p_lo >> 3)) sm_pack<X, T & 1, (p_lo >> 3)>(c);
  }
  if constexpr (HAS_S && HAS_PV) {
    constexpr int M0 = MX0;
    if constexpr (G >= M0 && G < M0 + 24) { constexpr int i = G - M0; mx_step<XN, i / 8, i % 8>(c); }
    if constexpr (G >= M0 + 9 && G < M0 + 9 + 24 && ((G - M0 - 9) % 8) == 0) mx_fin<(T + 1) & 1, (G - M0 - 9) / 8>(c);
  }
  GAP();
}
template <int T, int... Gs> DI void gaps(Ctx& c, iseq<Gs...>) { (gap<T, Gs>(c), ...); }
template <int X, int PAR, int... Is> DI void mx_all(Ctx& c, iseq<Is...>) { (mx_step<X, Is / 8, Is % 8>(c), ...); mx_fin<PAR, 0>(c); mx_fin<PAR, 1>(c); mx_fin<PAR, 2>(c); }

template <int T> DI void interval(Ctx& c) {
  constexpr bool HAS_S = T < 7, HAS_PV = T >= 1;
  constexpr int NG = (HAS_S ? 48 : 0) + (HAS_PV ? 48 : 0);
  if constexpr (T >= 7) {
    rd_p<(T - 1) & 1, 0, 0>(c, 0); rd_p<(T - 1) & 1, 0, 1>(c, 0); rd_p<(T - 1) & 1, 0, 2>(c, 0);
  }
  c.resc = false;
  gaps<T>(c, make_iseq<NG>{});
  if constexpr (HAS_PV && T <= 7) {
    if (__builtin_expect(c.resc, 0)) {
      asm volatile("s_nop 7\n\ts_nop 7" : "+a"(c.o[0][0]), "+a"(c.o[0][1]), "+a"(c.o[0][2]), "+a"(c.o[1][0]), "+a"(c.o[1][1]), "+a"(c.o[1][2]));
#pragma unroll
      for (int dt = 0; dt < 2; ++dt)
#pragma unroll
        for (int qt = 0; qt < 3; ++qt)
#pragma unroll
          for (int r = 0; r < 16; ++r) c.o[dt][qt][r] *= c.alpha[qt];
      asm volatile("s_nop 1" : "+a"(c.o[0][0]), "+a"(c.o[0][1]), "+a"(c.o[0][2]), "+a"(c.o[1][0]), "+a"(c.o[1][1]), "+a"(c.o[1][2]));
    }
  }
  if constexpr (HAS_S && !HAS_PV) {
    asm volatile("s_nop 7\n\ts_nop 7" : "+v"(c.s[(T & 1) ^ 1][0]), "+v"(c.s[(T & 1) ^ 1][1]), "+v"(c.s[(T & 1) ^ 1][2]));
    mx_all<(T & 1) ^ 1, (T + 1) & 1>(c, make_iseq<24>{});
  }
  if constexpr (T == 7) {
#pragma unroll
    for (int qt = 0; qt < 3; ++qt) {
      auto pr = __builtin_amdgcn_permlane32_swap(__float_as_uint(c.l[qt]), __float_as_uint(c.l[qt]), false, false);
      *(LDS_AS float*)(uintptr_t)(c.vMxw + (unsigned)(qt * 512)) = __uint_as_float(pr[0]) + __uint_as_float(pr[1]);
    }
  }
  barrier();
  GAP();
}

template <int Gr, int J> DI void qgap(Ctx& c) {
  constexpr int dt = J / 3, qt = J % 3;
  constexpr int ISS = NRING + 2 * Gr + (J > 1) + (J > 4);
  mfma_o<Gr == 0>(c.o[dt][qt], c.vf[Gr & 1][dt], c.pf[Gr & 1][qt]);
  if constexpr (Gr + 1 < 28) {
    constexpr int G1 = Gr + 1, cc1 = G1 >> 3, ks1 = G1 & 7;
    if constexpr (J == 0) { wait_gran<2 * G1, 2, ISS>(); rd_v<2 * G1, 0>(c, G1 & 1); }
    if constexpr (J == 2) rd_v<2 * G1, 1>(c, G1 & 1);
    if constexpr (ks1 != 0) {
      if constexpr (J == 3) { rd_p<cc1 & 1, ks1, 0>(c, G1 & 1); rd_p<cc1 & 1, ks1, 1>(c, G1 & 1); }
      if constexpr (J == 2) { rd_p<cc1 & 1, ks1, 2>(c, G1 & 1); }
    }
  } else {
    if constexpr (J == 0) { wait_gran<56, 4, ISS>(); rd_k<56, 0>(c, 0); }
    if constexpr (J == 2) rd_k<56, 1>(c, 0);
    if constexpr (J == 3) rd_k<56, 2>(c, 0);
    if constexpr (J == 5) rd_k<56, 3>(c, 0);
  }
  if constexpr (J == 1 || J == 4) issue_piece<2 * Gr + NRING + (J == 4)>(c, dep8(c.vf[Gr & 1][1]));
  GAP();
}
template <int Gr, int... Js> DI void qgran(Ctx& c, iseq<Js...>) { (qgap<Gr, Js>(c), ...); }

template <int I> DI void store_part_a(Ctx& c) {
  constexpr int t = I >> 1, j = I & 1, dt = t / 3, qt = t % 3;
  asm volatile("" : "+a"(c.o[dt][qt]));
  const f32x16& a = c.o[dt][qt];
  u32x4 v = {cvtpk(a[8 * j], a[8 * j + 1]), cvtpk(a[8 * j + 2], a[8 * j + 3]), cvtpk(a[8 * j + 4], a[8 * j + 5]), cvtpk(a[8 * j + 6], a[8 * j + 7])};
  __builtin_amdgcn_raw_buffer_store_b128(v, c.srdP, c.vpart, I * 1024, PSTORE_AUX);
}
template <int I> DI void store_part_b(Ctx& c) {
  constexpr int t = I >> 1, j = I & 1, dt = t / 3, qt = t % 3;
  const f32x16& a = c.s[dt][qt];
  u32x4 v = {cvtpk(a[8 * j], a[8 * j + 1]), cvtpk(a[8 * j + 2], a[8 * j + 3]), cvtpk(a[8 * j + 4], a[8 * j + 5]), cvtpk(a[8 * j + 6], a[8 * j + 7])};
  __builtin_amdgcn_raw_buffer_store_b128(v, c.srdP, c.vpart, (48 + I) * 1024, PSTORE_AUX);
}
template <int PASS, int Gr, int J> DI void ogap(Ctx& c) {
  constexpr int dt = J / 3, qt = J % 3, IB = 312 + 32 * PASS, GG = 16 * PASS + Gr;
  constexpr int ISS = NRING + IB + 2 * Gr + (J > 1) + (J > 4);
  constexpr int YS = (PASS == 1 && Gr > 4) ? Gr - 4 : 0;
  if constexpr (PASS == 0) mfma_o<Gr == 0>(c.o[dt][qt], c.vf[GG & 1][dt], c.pf[GG & 1][qt]);
  else mfma_v<Gr == 0>(c.s[dt][qt], c.vf[GG & 1][dt], c.pf[GG & 1][qt]);
  if constexpr (GG + 1 < 32) {
    constexpr int G1 = GG + 1, k1 = G1 & 15;
    if constexpr (J == 0) { wait_gran<312 + 2 * G1, 2, ISS, YS>(); rd_v<312 + 2 * G1, 0>(c, G1 & 1); }
    if constexpr (J == 2) { rd_v<312 + 2 * G1, 1>(c, G1 & 1); rd_p<(k1 >> 3), k1 & 7, 2>(c, G1 & 1); }
    if constexpr (J == 3) { rd_p<(k1 >> 3), k1 & 7, 0>(c, G1 & 1); rd_p<(k1 >> 3), k1 & 7, 1>(c, G1 & 1); }
  }
  if constexpr (J == 1 || J == 4) issue_piece<IB + 2 * Gr + NRING + (J == 4)>(c, dep8(c.vf[GG & 1][1]));
  if constexpr (PASS == 1 && Gr >= 4 && J == 5) store_part_a<Gr - 4>(c);
  GAP();
}
template <int PASS, int Gr, int... Js> DI void ogran(Ctx& c, iseq<Js...>) { (ogap<PASS, Gr, Js>(c), ...); }
template <int PASS, int... Gs> DI void opass(Ctx& c, iseq<Gs...>) { (ogran<PASS, Gs>(c, make_iseq<6>{}), ...); }
template <int... Is> DI void store_all_a(Ctx& c, iseq<Is...>) { (store_part_a<Is>(c), ...); }
template <int... Is> DI void store_all_b(Ctx& c, iseq<Is...>) { (store_part_b<Is>(c), ...); }

#define ST(i)
extern "C" __global__ __launch_bounds__(256, 1)
void attn_kernel(const unsigned short* __restrict__ x, const unsigned short* __restrict__ kc, const unsigned short* __restrict__ vc,
                 const unsigned short* __restrict__ wq, const unsigned short* __restrict__ wo, unsigned short* __restrict__ part,
                 unsigned long long* __restrict__ dbg) {
  extern __shared__ __attribute__((aligned(16))) char lds[];
  const unsigned ldsb = (unsigned)(uintptr_t)(LDS_AS char*)lds;
  const unsigned tid = threadIdx.x, lane = tid & 63u, l32 = lane & 31u, hi = lane >> 5;
  const unsigned w = __builtin_amdgcn_readfirstlane(tid >> 6);
  const unsigned bid = blockIdx.x, b = bid & 7u, idx = bid >> 3, h = idx >> 1, half = idx & 1u;
  const unsigned row0 = b * SQ + half * 96u;
  Ctx c;
  c.srdK = __builtin_amdgcn_make_buffer_rsrc((void*)(kc + (size_t)(b * NH + h) * SK * DH), (short)0, SK * DH * 2, 0x00020000);
  c.srdV = __builtin_amdgcn_make_buffer_rsrc((void*)(vc + (size_t)(b * NH + h) * SK * DH), (short)0, SK * DH * 2, 0x00020000);
  c.srdW = __builtin_amdgcn_make_buffer_rsrc((void*)wq, (short)0, DM * HD * 2, 0x00020000);
  c.srdX = __builtin_amdgcn_make_buffer_rsrc((void*)x, (short)0, NB * SQ * DM * 2, 0x00020000);
  {
    const unsigned a = lane >> 3, p7 = lane & 7u;
    c.voffK[0] = a * 512u + ((p7 ^ (a >> 1)) * 16u);
    c.voffK[1] = a * 512u + ((p7 ^ (a >> 1) ^ 4u) * 16u);
    c.voffV = ((a & 3u) + 8u * (a >> 2)) * 512u + ((p7 ^ ((a & 2u) << 1)) * 16u);
    c.voffW = a * 8192u + ((p7 ^ ((a & 2u) << 1)) * 16u);
    c.sK = w * 32u * 512u; c.sV = w * 128u; c.sW = h * 512u + w * 128u;
    c.ringM0 = ldsb + LDS_RING + w * (NRING * 1024u);
    c.vK = c.ringM0 + l32 * 128u + ((hi ^ ((l32 >> 1) & 7u)) * 16u);
    const unsigned g16 = lane >> 4, dblk = g16 & 1u, i16 = lane & 15u, q4 = i16 >> 2, p4 = i16 & 3u;
    c.vV = c.ringM0 + (8u * hi + q4) * 128u + (((2u * dblk + (p4 >> 1)) ^ ((q4 >> 1) << 2)) * 16u) + 8u * (p4 & 1u);
    c.vP = ldsb + LDS_P + l32 * 256u + ((hi ^ (l32 & 15u)) * 16u);
    c.vPw = ldsb + LDS_P + l32 * 256u + (((4u * w + hi) ^ (l32 & 15u)) * 16u);
    c.vMx = ldsb + LDS_MX + l32 * 16u; c.vMxw = c.vMx + w * 4u;
  }
  c.srdO = __builtin_amdgcn_make_buffer_rsrc((void*)wo, (short)0, HD * DM * 2, 0x00020000);
  c.voffO = (lane >> 3) * 896u + (((lane & 7u) ^ (((lane >> 3) & 2u) << 1)) * 16u); c.sO = h * 256u * 896u + w * 128u;
  c.srdP = __builtin_amdgcn_make_buffer_rsrc((void*)(part + (size_t)((b * NH + h) * 2u + half) * 84u * 512u), (short)0, 84 * 1024, 0x00020000);
  c.vpart = w * 12u * 1024u + lane * 16u;
  auto load_x = [&](int cc) {
#pragma unroll
    for (int i = 0; i < 6; ++i) {
      const unsigned pc = 6u * w + i;
      const unsigned voff = (lane >> 4) * 896u + ((((lane & 15u) ^ (lane >> 4)) ^ (4u * (pc & 3u))) * 16u);
      dma(ldsb + LDS_P + (unsigned)(cc & 1) * 24576u + pc * 1024u, voff, c.srdX, (row0 + 4u * pc) * 896u + (unsigned)cc * 256u);
    }
  };
  ST(0);
  load_x(0);
  [&]<int... Is>(iseq<Is...>) { (issue_piece<Is>(c, 0u), ...); }(make_iseq<NRING>{});
  load_x(1);
  {
    const unsigned scr = ldsb + LDS_SCR + w * 256u;
    const unsigned g = (blockIdx.x * 4u + w) * 16u + (lane & 15u);
    const unsigned woff = (lane < 16u) ? (192u + (g >> 6)) * 8192u + (g & 63u) * 128u : 0xFFFFFF00u;
    asm volatile("s_mov_b32 m0, %0\n\ts_nop 0\n\tbuffer_load_dword %1, %2, 0 offen lds" :: "s"(scr), "v"(woff), "s"(c.srdW) : "memory");
    const unsigned xg = (blockIdx.x * 4u + w) * 5u + lane;
    const unsigned xr = xg / 3u, xc = xg - 3u * xr;
    const unsigned xoff = (lane < 5u && xg < 4608u) ? xr * 896u + 512u + xc * 128u : 0xFFFFFF00u;
    asm volatile("s_mov_b32 m0, %0\n\ts_nop 0\n\tbuffer_load_dword %1, %2, 0 offen lds" :: "s"(scr), "v"(xoff), "s"(c.srdX) : "memory");
  }
  wait_vm<NRING + 6 + 2>();
  barrier();
  ST(1);
  wait_gran<0, 2, NRING>(); rd_v<0, 0>(c, 0); rd_v<0, 1>(c, 0);
  rd_p<0, 0, 0>(c, 0); rd_p<0, 0, 1>(c, 0); rd_p<0, 0, 2>(c, 0);
  GAP();
  [&]<int... Gs>(iseq<Gs...>) {
    ([&] {
      qgran<Gs>(c, make_iseq<6>{});
      if constexpr ((Gs & 7) == 7) {
        constexpr int cc1 = (Gs + 1) >> 3;
        wait_vm<14>();
        barrier();
        rd_p<cc1 & 1, 0, 0>(c, (Gs + 1) & 1); rd_p<cc1 & 1, 0, 1>(c, (Gs + 1) & 1); rd_p<cc1 & 1, 0, 2>(c, (Gs + 1) & 1);
        if constexpr (cc1 + 1 < 4) load_x(cc1 + 1);
        GAP();
      }
    }(), ...);
  }(make_iseq<28>{});
  barrier();
  ST(2);
  asm volatile("s_nop 7\n\ts_nop 7" : "+a"(c.o[0][0]), "+a"(c.o[0][1]), "+a"(c.o[0][2]), "+a"(c.o[1][0]), "+a"(c.o[1][1]), "+a"(c.o[1][2]));
#pragma unroll
  for (int qt = 0; qt < 3; ++qt)
#pragma unroll
    for (int dt = 0; dt < 2; ++dt)
#pragma unroll
      for (int g = 0; g < 4; ++g) {
        u32x2 v = {cvtpk(c.o[dt][qt][4 * g], c.o[dt][qt][4 * g + 1]), cvtpk(c.o[dt][qt][4 * g + 2], c.o[dt][qt][4 * g + 3])};
        const unsigned ci = 8u * w + 4u * dt + g;
        *(LDS_AS u32x2*)(uintptr_t)(ldsb + LDS_P + (32u * qt + l32) * 512u + ((ci ^ (l32 & 15u)) * 16u) + 8u * hi) = v;
      }
  barrier();
#pragma unroll
  for (int qt = 0; qt < 3; ++qt)
#pragma unroll
    for (int ks = 0; ks < 16; ++ks)
      c.q[qt * 16 + ks] = lds_b128(ldsb + LDS_P + (32u * qt + l32) * 512u + (((2u * ks + hi) ^ (l32 & 15u)) * 16u));
  GAP();
  ST(3);
  interval<-1>(c);
  ST(4);
  {
#pragma unroll
    for (int qt = 0; qt < 3; ++qt) {
      f32x4 v = *(const LDS_AS f32x4*)(uintptr_t)(c.vMx + (unsigned)(qt * 512));
      c.m[qt] = fmaxf(fmaxf(v[0], v[1]), fmaxf(v[2], v[3])); c.l[qt] = 0.f; c.mc[qt] = -c.m[qt] * SCL;
    }
  }
  GAP();
  interval<0>(c); ST(5); interval<1>(c); ST(6); interval<2>(c); ST(7); interval<3>(c); ST(8); interval<4>(c); ST(9);
  interval<5>(c); ST(10); interval<6>(c); ST(11); interval<7>(c); ST(12);
  interval<8>(c); ST(13);
  asm volatile("s_nop 7\n\ts_nop 7" : "+a"(c.o[0][0]), "+a"(c.o[0][1]), "+a"(c.o[0][2]), "+a"(c.o[1][0]), "+a"(c.o[1][1]), "+a"(c.o[1][2]));
#pragma unroll
  for (int qt = 0; qt < 3; ++qt) {
    const float inv = c.alpha[qt];
#pragma unroll
    for (int dt = 0; dt < 2; ++dt)
#pragma unroll
      for (int g = 0; g < 4; ++g) {
        u32x2 o2 = {cvtpk(c.o[dt][qt][4 * g] * inv, c.o[dt][qt][4 * g + 1] * inv), cvtpk(c.o[dt][qt][4 * g + 2] * inv, c.o[dt][qt][4 * g + 3] * inv)};
        const unsigned ci = 8u * (w & 1u) + 4u * dt + g;
        *(LDS_AS u32x2*)(uintptr_t)(ldsb + LDS_P + (w >> 1) * 24576u + (32u * qt + l32) * 256u + ((ci ^ (l32 & 15u)) * 16u) + 8u * hi) = o2;
      }
  }
  barrier();
  rd_p<0, 0, 0>(c, 0); rd_p<0, 0, 1>(c, 0); rd_p<0, 0, 2>(c, 0);
  GAP();
  opass<0>(c, make_iseq<16>{});
  asm volatile("s_nop 7\n\ts_nop 7" : "+a"(c.o[0][0]), "+a"(c.o[0][1]), "+a"(c.o[0][2]), "+a"(c.o[1][0]), "+a"(c.o[1][1]), "+a"(c.o[1][2]));
  if (w < 3u) {
    opass<1>(c, make_iseq<16>{});
    asm volatile("s_nop 7\n\ts_nop 7" : "+v"(c.s[0][0]), "+v"(c.s[0][1]), "+v"(c.s[0][2]), "+v"(c.s[1][0]), "+v"(c.s[1][1]), "+v"(c.s[1][2]));
    store_all_b(c, make_iseq<12>{});
  } else {
    asm volatile("s_nop 7\n\ts_nop 7" : "+a"(c.o[0][0]), "+a"(c.o[0][1]), "+a"(c.o[0][2]), "+a"(c.o[1][0]), "+a"(c.o[1][1]), "+a"(c.o[1][2]));
    store_all_a(c, make_iseq<12>{});
  }
  ST(14);
}

extern "C" __global__ __launch_bounds__(256)
void reduce_kernel(const unsigned short* __restrict__ part, unsigned short* __restrict__ out) {
  const unsigned idx = blockIdx.x * 256u + threadIdx.x, lane = idx & 63u, rest = idx >> 6;
  const unsigned blk = rest % 84u, bh2 = rest / 84u, half = bh2 & 1u, b = bh2 >> 1;
  const u32x4* p = (const u32x4*)(part + ((size_t)((b * NH) * 2u + half) * 84u + blk) * 512u + lane * 8u);
  u32x4 v[16];
#pragma unroll
  for (int h = 0; h < 16; ++h) v[h] = p[(size_t)h * (2u * 84u * 64u)];
  float acc[8] = {0.f, 0.f, 0.f, 0.f, 0.f, 0.f, 0.f, 0.f};
#pragma unroll
  for (int h = 0; h < 16; ++h)
#pragma unroll
    for (int e = 0; e < 4; ++e) { acc[2 * e] += __uint_as_float(v[h][e] << 16); acc[2 * e + 1] += __uint_as_float(v[h][e] & 0xFFFF0000u); }
  const unsigned slice = blk / 12u, tj = blk - 12u * slice, tile = tj >> 1, j = tj & 1u, dt = tile / 3u, qt = tile - 3u * dt;
  const unsigned row = b * SQ + half * 96u + 32u * qt + (lane & 31u), n0 = 64u * slice + 32u * dt + 16u * j + 4u * (lane >> 5);
  u32x2 o0 = {cvtpk(acc[0], acc[1]), cvtpk(acc[2], acc[3])}, o1 = {cvtpk(acc[4], acc[5]), cvtpk(acc[6], acc[7])};
  *(u32x2*)(out + (size_t)row * DM + n0) = o0;
  *(u32x2*)(out + (size_t)row * DM + n0 + 8u) = o1;
}

extern "C" void kernel_launch(void* const* d_in, const int* in_sizes, int n_in, void* d_out, int out_size, void* d_ws, size_t ws_size, hipStream_t stream) {
  const unsigned short* x = (const unsigned short*)d_in[0]; const unsigned short* kc = (const unsigned short*)d_in[1];
  const unsigned short* vc = (const unsigned short*)d_in[2]; const unsigned short* wq = (const unsigned short*)d_in[3];
  const unsigned short* wo = (const unsigned short*)d_in[4];
  char* ws = (char*)d_ws;
  unsigned short* part = (unsigned short*)ws;
  (void)hipFuncSetAttribute((const void*)attn_kernel, hipFuncAttributeMaxDynamicSharedMemorySize, (int)LDS_TOTAL);
  attn_kernel<<<256, 256, LDS_TOTAL, stream>>>(x, kc, vc, wq, wo, part, (unsigned long long*)(ws + (64u << 20)));
  reduce_kernel<<<336, 256, 0, stream>>>(part, (unsigned short*)d_out);
}
```

```cpp
#include <hip/hip_runtime.h>
#include <hip/hip_bf16.h>
#include <stdint.h>
#include <utility>

typedef __bf16 bf16x8 __attribute__((ext_vector_type(8)));
typedef __bf16 bf16x4 __attribute__((ext_vector_type(4)));
typedef __bf16 bf16x2 __attribute__((ext_vector_type(2)));
typedef float f32x16 __attribute__((ext_vector_type(16)));
typedef float f32x4 __attribute__((ext_vector_type(4)));
typedef float f32x2 __attribute__((ext_vector_type(2)));
typedef unsigned u32x4 __attribute__((ext_vector_type(4)));
typedef unsigned u32x2 __attribute__((ext_vector_type(2)));
#define DI __device__ __forceinline__
#define LDS_AS __attribute__((address_space(3)))
#define GAP() __builtin_amdgcn_sched_barrier(0)
template <int... Is> using iseq = std::integer_sequence<int, Is...>;
template <int N> using make_iseq = std::make_integer_sequence<int, N>;

constexpr int NB = 8, SQ = 192, DM = 448, NH = 16, SK = 1024, DH = 256, HD = NH * DH;
constexpr int NRING = 24;
constexpr int NPIECE = 376;
constexpr unsigned LDS_RING = 0, LDS_P = 4 * NRING * 1024, LDS_MX = LDS_P + 49152, LDS_SCR = LDS_MX + 3072, LDS_TOTAL = 163840;
constexpr float SCL = 0.0625f * 1.4426950408889634f;
constexpr float THR = 8.0f;
constexpr int SM1_96 = 60, MX0 = 58;
constexpr int PSTORE_AUX = 16;
constexpr int NQA = 40;

struct Piece { int type, tile, p; };
constexpr Piece piece_of(int idx) {
  if (idx < 56) return Piece{0, idx >> 1, idx & 1};
  int j = idx - 56;
  if (j < 32) return Piece{1, j >> 4, j & 15};
  j -= 32;
  if (j < 192) { int blk = j >> 5, r = j & 31; return r < 16 ? Piece{1, blk + 2, r} : Piece{2, blk, r - 16}; }
  j -= 192;
  if (j < 32) return Piece{2, 6 + (j >> 4), j & 15};
  j -= 32;
  if (j < 64) return Piece{4, j >> 1, j & 1};
  return Piece{3, 0, 0};
}
constexpr int imin(int a, int b) { return a < b ? a : b; }

struct Ctx {
  __amdgpu_buffer_rsrc_t srdK, srdV, srdW, srdX, srdO;
  unsigned voffO, sO;
  unsigned voffK[2], voffV, voffW;
  unsigned sK, sV, sW;
  unsigned ringM0;
  unsigned vK, vV, vP, vPw, vMx, vMxw;
  bf16x8 q[48];
  f32x16 o[2][3];
  f32x16 s[2][3];
  bf16x8 kf[2][4], vf[2][2], pf[2][3];
  float m[3], l[3], mc[3], alpha[3], mxr[3]; f32x4 mxv[3]; bool resc;
  __amdgpu_buffer_rsrc_t srdP; unsigned vpart;
};

DI unsigned cvtpk(float lo, float hi) { f32x2 v = {lo, hi}; bf16x2 b = __builtin_convertvector(v, bf16x2); return __builtin_bit_cast(unsigned, b); }
DI float max3f(float a, float b, float c) { float r; asm("v_max3_f32 %0, %1, %2, %3" : "=v"(r) : "v"(a), "v"(b), "v"(c)); return r; }
DI float max2f(float a, float b) { float r; asm("v_max_f32_e32 %0, %1, %2" : "=v"(r) : "v"(a), "v"(b)); return r; }
DI float fadd_s(float a, float b) { float r; asm("v_add_f32_e32 %0, %1, %2" : "=v"(r) : "v"(a), "v"(b)); return r; }
template <int N> DI void wait_vm() { asm volatile("s_waitcnt vmcnt(%0)" :: "i"(N) : "memory"); }
DI void barrier() { asm volatile("s_waitcnt lgkmcnt(0)\n\ts_barrier" ::: "memory"); }
DI void dma(unsigned m0, unsigned voff, __amdgpu_buffer_rsrc_t srd, unsigned soff) {
  asm volatile("s_mov_b32 m0, %0\n\ts_nop 0\n\tbuffer_load_dwordx4 %1, %2, %3 offen lds" :: "s"(m0), "v"(voff), "s"(srd), "s"(soff) : "memory");
}
DI void dma_dep(unsigned m0, unsigned voff, __amdgpu_buffer_rsrc_t srd, unsigned soff, unsigned dep) {
  asm volatile("s_mov_b32 m0, %0\n\ts_nop 0\n\tbuffer_load_dwordx4 %1, %2, %3 offen lds" :: "s"(m0), "v"(voff), "s"(srd), "s"(soff), "v"(dep) : "memory");
}
template <unsigned M0OFF, unsigned SOFF> DI void dma_dep_nt(unsigned m0b, unsigned voff, __amdgpu_buffer_rsrc_t srd, unsigned sb, unsigned dep) {
  unsigned tmp;
  asm volatile("s_add_u32 m0, %1, %6\n\ts_add_u32 %0, %4, %7\n\tbuffer_load_dwordx4 %2, %3, %0 offen nt lds" : "=&s"(tmp) : "s"(m0b), "v"(voff), "s"(srd), "s"(sb), "v"(dep), "i"(M0OFF), "i"(SOFF) : "memory", "scc");
}
template <int IDX> DI void issue_piece(const Ctx& c, unsigned dep) {
  constexpr Piece pc = piece_of(IDX);
  if constexpr (pc.type != 3) {
    const unsigned m0 = c.ringM0 + (unsigned)(IDX % NRING) * 1024u;
    if constexpr (pc.type == 0) dma_dep(m0, c.voffW, c.srdW, c.sW + (unsigned)(16 * pc.tile + 8 * pc.p) * 8192u, dep);
    else if constexpr (pc.type == 1) dma_dep_nt<(unsigned)(IDX % NRING) * 1024u, (unsigned)(128 * pc.tile + 8 * (pc.p & 3)) * 512u + (unsigned)(pc.p >> 2) * 128u>(c.ringM0, c.voffK[pc.p & 1], c.srdK, c.sK, dep);
    else if constexpr (pc.type == 2) dma_dep_nt<(unsigned)(IDX % NRING) * 1024u, (unsigned)(128 * pc.tile + 16 * (pc.p >> 1) + 4 * (pc.p & 1)) * 512u>(c.ringM0, c.voffV, c.srdV, c.sV, dep);
    else dma_dep(m0, c.voffO, c.srdO, c.sO + (unsigned)(pc.tile >> 4) * 512u + (unsigned)(16 * (pc.tile & 15) + 8 * pc.p) * 896u, dep);
  }
}
DI bf16x8 lds_b128(unsigned addr) { return *(const LDS_AS bf16x8*)(uintptr_t)addr; }
DI bf16x4 lds_tr(unsigned addr) { return __builtin_amdgcn_ds_read_tr16_b64_v4bf16((LDS_AS bf16x4*)(uintptr_t)addr); }
DI unsigned dep8(const bf16x8& v) { return __builtin_bit_cast(u32x4, v)[3]; }

template <int QI, bool ZERO> DI void mfma_s(f32x16& s, const bf16x8& kf, const bf16x8& qf) {
  if constexpr (QI < NQA) {
    if constexpr (ZERO) asm volatile("v_mfma_f32_32x32x16_bf16 %0, %1, %2, 0" : "=&v"(s) : "v"(kf), "a"(qf));
    else                asm volatile("v_mfma_f32_32x32x16_bf16 %0, %1, %2, %0" : "+v"(s) : "v"(kf), "a"(qf));
  } else {
    if constexpr (ZERO) asm volatile("v_mfma_f32_32x32x16_bf16 %0, %1, %2, 0" : "=&v"(s) : "v"(kf), "v"(qf));
    else                asm volatile("v_mfma_f32_32x32x16_bf16 %0, %1, %2, %0" : "+v"(s) : "v"(kf), "v"(qf));
  }
}
template <bool ZERO> DI void mfma_o(f32x16& o, const bf16x8& vf, const bf16x8& pf) {
  if constexpr (ZERO) asm volatile("v_mfma_f32_32x32x16_bf16 %0, %1, %2, 0" : "=&a"(o) : "v"(vf), "v"(pf));
  else                asm volatile("v_mfma_f32_32x32x16_bf16 %0, %1, %2, %0" : "+a"(o) : "v"(vf), "v"(pf));
}

template <bool ZERO> DI void mfma_v(f32x16& o, const bf16x8& vf, const bf16x8& pf) {
  if constexpr (ZERO) asm volatile("v_mfma_f32_32x32x16_bf16 %0, %1, %2, 0" : "=&v"(o) : "v"(vf), "v"(pf));
  else                asm volatile("v_mfma_f32_32x32x16_bf16 %0, %1, %2, %0" : "+v"(o) : "v"(vf), "v"(pf));
}

template <int I0, int S> DI void rd_k(Ctx& c, int buf) { c.kf[buf][S] = lds_b128((c.vK ^ (32u * S)) + (unsigned)(I0 % NRING) * 1024u); }
template <int I0, int DT> DI void rd_v(Ctx& c, int buf) {
  bf16x4 lo = lds_tr((c.vV ^ (64u * DT)) + (unsigned)(I0 % NRING) * 1024u);
  bf16x4 hi = lds_tr((c.vV ^ (64u * DT)) + (unsigned)(I0 % NRING) * 1024u + 512u);
  c.vf[buf][DT] = __builtin_shufflevector(lo, hi, 0, 1, 2, 3, 4, 5, 6, 7);
}
template <int BUF, int KS, int QT> DI void rd_p(Ctx& c, int buf) { c.pf[buf][QT] = lds_b128((c.vP ^ (32u * KS)) + (unsigned)(QT * 8192 + BUF * 24576)); }
constexpr int NEXTRA = 8;
template <int I0, int G, int ISSUED, int YOUNGER = 0> DI void wait_gran() {
  static_assert(imin(NPIECE, ISSUED) >= I0 + G, "not issued yet");
  constexpr int n = imin(NPIECE, ISSUED) - (I0 + G) + ((I0 + G <= NRING) ? NEXTRA : 0) + YOUNGER;
  wait_vm<(n > 63 ? 63 : n)>();
}
constexpr int base_of(int T) { return (T == -1) ? 56 : (T == 0) ? 72 : (T <= 6) ? 88 + 32 * (T - 1) : 280 + 16 * (T - 7); }
constexpr int issued_at(int T, int G) { return NRING + base_of(T) + (G + 1) / 3; }

constexpr int ik_of(int T) { return (T == -1) ? 56 : (T == 0) ? 72 : 88 + 32 * (T - 1); }
constexpr int iv_of(int T) { return (T <= 6) ? 88 + 32 * (T - 1) + 16 : 280 + 16 * (T - 7); }

template <int X, int E> DI void sm_exp(Ctx& c) { c.s[X][E >> 4][E & 15] = __builtin_amdgcn_exp2f(fmaf(c.s[X][E >> 4][E & 15], SCL, c.mc[E >> 4])); }
template <int X, int E> DI void sm_add(Ctx& c) { c.l[E >> 4] = fadd_s(c.l[E >> 4], c.s[X][E >> 4][E & 15]); }
template <int X, int BUF, int CH> DI void sm_pack(Ctx& c) {
  constexpr int qt = CH >> 1, ss = CH & 1;
  const f32x16& p = c.s[X][qt];
  u32x4 v = {cvtpk(p[8 * ss], p[8 * ss + 1]), cvtpk(p[8 * ss + 2], p[8 * ss + 3]), cvtpk(p[8 * ss + 4], p[8 * ss + 5]), cvtpk(p[8 * ss + 6], p[8 * ss + 7])};
  *(LDS_AS u32x4*)(uintptr_t)((c.vPw ^ (32u * ss)) + (unsigned)(qt * 8192 + BUF * 24576)) = v;
}
template <int X, int QT, int I> DI void mx_step(Ctx& c) {
  if constexpr (I == 0) c.mxr[QT] = max2f(c.s[X][QT][0], c.s[X][QT][1]);
  else c.mxr[QT] = max3f(c.mxr[QT], c.s[X][QT][2 * I], c.s[X][QT][2 * I + 1]);
}
template <int PAR, int QT> DI void mx_fin(Ctx& c) {
  auto pr = __builtin_amdgcn_permlane32_swap(__float_as_uint(c.mxr[QT]), __float_as_uint(c.mxr[QT]), false, false);
  const float mx = max2f(__uint_as_float(pr[0]), __uint_as_float(pr[1]));
  *(LDS_AS float*)(uintptr_t)(c.vMxw + (unsigned)(PAR * 1536 + QT * 512)) = mx;
}

template <int T, int G> DI void gap(Ctx& c) {
  constexpr bool HAS_S = T < 7, HAS_SM = T >= 0 && T < 8, HAS_PV = T >= 1;
  constexpr int NS = HAS_S ? 48 : 0, NG = NS + (HAS_PV ? 48 : 0);
  constexpr int X = T & 1, XN = X ^ 1;
  constexpr int IK = ik_of(T), IV = iv_of(T);
  if constexpr (G < NS) {
    constexpr int g = G / 12, s = (G % 12) / 3, qt = G % 3;
    mfma_s<qt * 16 + 4 * g + s, (g == 0 && s == 0)>(c.s[XN][qt], c.kf[g & 1][s], c.q[qt * 16 + 4 * g + s]);
  } else {
    constexpr int P = G - NS, ks = P / 6, dt = (P % 6) / 3, qt = P % 3;
    mfma_o<(T == 1 && ks == 0)>(c.o[dt][qt], c.vf[ks & 1][dt], c.pf[ks & 1][qt]);
  }
  constexpr int ISS = issued_at(T, G);
  if constexpr (G < NS) {
    constexpr int g = G / 12, j = G % 12;
    if constexpr (g < 3) {
      constexpr int I0 = IK + 4 * (g + 1);
      if constexpr (j == 0) { wait_gran<I0, 4, ISS>(); rd_k<I0, 0>(c, (g + 1) & 1); }
      if constexpr (j == 2) rd_k<I0, 1>(c, (g + 1) & 1);
      if constexpr (j == 3) rd_k<I0, 2>(c, (g + 1) & 1);
      if constexpr (j == 5) rd_k<I0, 3>(c, (g + 1) & 1);
    } else if constexpr (HAS_PV) {
      if constexpr (j == 0) { wait_gran<IV, 2, ISS>(); rd_v<IV, 0>(c, 0); }
      if constexpr (j == 2) rd_v<IV, 1>(c, 0);
      if constexpr (j == 3) rd_p<(T - 1) & 1, 0, 0>(c, 0);
      if constexpr (j == 5) rd_p<(T - 1) & 1, 0, 1>(c, 0);
      if constexpr (j == 6) rd_p<(T - 1) & 1, 0, 2>(c, 0);
    } else {
      constexpr int I0 = ik_of(T + 1);
      if constexpr (j == 0) { wait_gran<I0, 4, ISS>(); rd_k<I0, 0>(c, 0); }
      if constexpr (j == 2) rd_k<I0, 1>(c, 0);
      if constexpr (j == 3) rd_k<I0, 2>(c, 0);
      if constexpr (j == 5) rd_k<I0, 3>(c, 0);
    }
    if constexpr (G % 3 == 1) issue_piece<base_of(T) + NRING + G / 3>(c, dep8(c.kf[g & 1][3]));
  } else {
    constexpr int P = G - NS, ks = P / 6, j = P % 6;
    if constexpr (ks < 7) {
      constexpr int I0 = IV + 2 * (ks + 1);
      if constexpr (j == 0) { wait_gran<I0, 2, ISS>(); rd_v<I0, 0>(c, (ks + 1) & 1); }
      if constexpr (j == 2) { rd_v<I0, 1>(c, (ks + 1) & 1); }
      if constexpr (j == 3) { rd_p<(T - 1) & 1, ks + 1, 0>(c, (ks + 1) & 1); rd_p<(T - 1) & 1, ks + 1, 1>(c, (ks + 1) & 1); }
      if constexpr (j == 4) { rd_p<(T - 1) & 1, ks + 1, 2>(c, (ks + 1) & 1); }
    } else if constexpr (T < 6) {
      constexpr int I0 = ik_of(T + 1);
      if constexpr (j == 0) { wait_gran<I0, 4, ISS>(); rd_k<I0, 0>(c, 0); }
      if constexpr (j == 2) rd_k<I0, 1>(c, 0);
      if constexpr (j == 3) rd_k<I0, 2>(c, 0);
      if constexpr (j == 5) rd_k<I0, 3>(c, 0);
    } else {
      constexpr int I0 = (T < 8) ? iv_of(T + 1) : 312;
      if constexpr (j == 0) { wait_gran<I0, 2, ISS>(); rd_v<I0, 0>(c, 0); }
      if constexpr (j == 2) rd_v<I0, 1>(c, 0);
    }
    if constexpr (G % 3 == 1) issue_piece<base_of(T) + NRING + G / 3>(c, dep8(c.vf[ks & 1][1]));
  }
  if constexpr (T == 8 && G == 1) {
#pragma unroll
    for (int qt = 0; qt < 3; ++qt) c.mxv[qt] = *(const LDS_AS f32x4*)(uintptr_t)(c.vMx + (unsigned)(qt * 512));
  }
  if constexpr (T == 8 && G == 4) {
#pragma unroll
    for (int qt = 0; qt < 3; ++qt) c.alpha[qt] = 1.0f / ((c.mxv[qt][0] + c.mxv[qt][1]) + (c.mxv[qt][2] + c.mxv[qt][3]));
  }
  if constexpr (HAS_SM && T >= 1) {
    if constexpr (G == 0) {
#pragma unroll
      for (int qt = 0; qt < 3; ++qt) c.mxv[qt] = *(const LDS_AS f32x4*)(uintptr_t)(c.vMx + (unsigned)((T & 1) * 1536 + qt * 512));
    }
    if constexpr (G == 2) {
      bool need = false;
#pragma unroll
      for (int qt = 0; qt < 3; ++qt) { c.mxr[qt] = max2f(max3f(c.mxv[qt][0], c.mxv[qt][1], c.mxv[qt][2]), c.mxv[qt][3]); need |= (c.mxr[qt] - c.m[qt]) * SCL > THR; }
      c.resc = __any(need);
      if (__builtin_expect(c.resc, 0)) {
#pragma unroll
        for (int qt = 0; qt < 3; ++qt) { const float mn = fmaxf(c.m[qt], c.mxr[qt]); c.alpha[qt] = __builtin_amdgcn_exp2f((c.m[qt] - mn) * SCL); c.m[qt] = mn; c.l[qt] *= c.alpha[qt]; c.mc[qt] = -mn * SCL; }
      }
    }
  }
  if constexpr (HAS_SM) {
    constexpr int SM0 = 3, SM1 = (NG == 96) ? SM1_96 : 44;
    constexpr int e_lo = (G <= SM0) ? 0 : (G >= SM1) ? 48 : ((G - SM0) * 48) / (SM1 - SM0);
    constexpr int e_hi = (G + 1 <= SM0) ? 0 : (G + 1 >= SM1) ? 48 : ((G + 1 - SM0) * 48) / (SM1 - SM0);
    constexpr int p_lo = (G - 1 <= SM0) ? 0 : (G - 1 >= SM1) ? 48 : ((G - 1 - SM0) * 48) / (SM1 - SM0);
    constexpr int p_hi = e_lo;
    if constexpr (p_lo + 0 < p_hi) sm_add<X, p_lo + 0>(c);
    if constexpr (p_lo + 1 < p_hi) sm_add<X, p_lo + 1>(c);
    if constexpr (e_lo + 0 < e_hi) sm_exp<X, e_lo + 0>(c);
    if constexpr (e_lo + 1 < e_hi) sm_exp<X, e_lo + 1>(c);
    if constexpr ((p_hi >> 3) > (p_lo >> 3)) sm_pack<X, T & 1, (p_lo >> 3)>(c);
  }
  if constexpr (HAS_S && HAS_PV) {
    constexpr int M0 = MX0;
    if constexpr (G >= M0 && G < M0 + 24) { constexpr int i = G - M0; mx_step<XN, i / 8, i % 8>(c); }
    if constexpr (G >= M0 + 9 && G < M0 + 9 + 24 && ((G - M0 - 9) % 8) == 0) mx_fin<(T + 1) & 1, (G - M0 - 9) / 8>(c);
  }
  GAP();
}
template <int T, int... Gs> DI void gaps(Ctx& c, iseq<Gs...>) { (gap<T, Gs>(c), ...); }
template <int X, int PAR, int... Is> DI void mx_all(Ctx& c, iseq<Is...>) { (mx_step<X, Is / 8, Is % 8>(c), ...); mx_fin<PAR, 0>(c); mx_fin<PAR, 1>(c); mx_fin<PAR, 2>(c); }

template <int T> DI void interval(Ctx& c) {
  constexpr bool HAS_S = T < 7, HAS_PV = T >= 1;
  constexpr int NG = (HAS_S ? 48 : 0) + (HAS_PV ? 48 : 0);
  if constexpr (T >= 7) {
    rd_p<(T - 1) & 1, 0, 0>(c, 0); rd_p<(T - 1) & 1, 0, 1>(c, 0); rd_p<(T - 1) & 1, 0, 2>(c, 0);
  }
  c.resc = false;
  gaps<T>(c, make_iseq<NG>{});
  if constexpr (HAS_PV && T <= 7) {
    if (__builtin_expect(c.resc, 0)) {
      asm volatile("s_nop 7\n\ts_nop 7" : "+a"(c.o[0][0]), "+a"(c.o[0][1]), "+a"(c.o[0][2]), "+a"(c.o[1][0]), "+a"(c.o[1][1]), "+a"(c.o[1][2]));
#pragma unroll
      for (int dt = 0; dt < 2; ++dt)
#pragma unroll
        for (int qt = 0; qt < 3; ++qt)
#pragma unroll
          for (int r = 0; r < 16; ++r) c.o[dt][qt][r] *= c.alpha[qt];
      asm volatile("s_nop 1" : "+a"(c.o[0][0]), "+a"(c.o[0][1]), "+a"(c.o[0][2]), "+a"(c.o[1][0]), "+a"(c.o[1][1]), "+a"(c.o[1][2]));
    }
  }
  if constexpr (HAS_S && !HAS_PV) {
    asm volatile("s_nop 7\n\ts_nop 7" : "+v"(c.s[(T & 1) ^ 1][0]), "+v"(c.s[(T & 1) ^ 1][1]), "+v"(c.s[(T & 1) ^ 1][2]));
    mx_all<(T & 1) ^ 1, (T + 1) & 1>(c, make_iseq<24>{});
  }
  if constexpr (T == 7) {
#pragma unroll
    for (int qt = 0; qt < 3; ++qt) {
      auto pr = __builtin_amdgcn_permlane32_swap(__float_as_uint(c.l[qt]), __float_as_uint(c.l[qt]), false, false);
      *(LDS_AS float*)(uintptr_t)(c.vMxw + (unsigned)(qt * 512)) = __uint_as_float(pr[0]) + __uint_as_float(pr[1]);
    }
  }
  barrier();
  GAP();
}

template <int Gr, int J> DI void qgap(Ctx& c) {
  constexpr int dt = J / 3, qt = J % 3;
  constexpr int ISS = NRING + 2 * Gr + (J > 1) + (J > 4);
  mfma_o<Gr == 0>(c.o[dt][qt], c.vf[Gr & 1][dt], c.pf[Gr & 1][qt]);
  if constexpr (Gr + 1 < 28) {
    constexpr int G1 = Gr + 1, cc1 = G1 >> 3, ks1 = G1 & 7;
    if constexpr (J == 0) { wait_gran<2 * G1, 2, ISS>(); rd_v<2 * G1, 0>(c, G1 & 1); }
    if constexpr (J == 2) rd_v<2 * G1, 1>(c, G1 & 1);
    if constexpr (ks1 != 0) {
      if constexpr (J == 3) { rd_p<cc1 & 1, ks1, 0>(c, G1 & 1); rd_p<cc1 & 1, ks1, 1>(c, G1 & 1); }
      if constexpr (J == 2) { rd_p<cc1 & 1, ks1, 2>(c, G1 & 1); }
    }
  } else {
    if constexpr (J == 0) { wait_gran<56, 4, ISS>(); rd_k<56, 0>(c, 0); }
    if constexpr (J == 2) rd_k<56, 1>(c, 0);
    if constexpr (J == 3) rd_k<56, 2>(c, 0);
    if constexpr (J == 5) rd_k<56, 3>(c, 0);
  }
  if constexpr (J == 1 || J == 4) issue_piece<2 * Gr + NRING + (J == 4)>(c, dep8(c.vf[Gr & 1][1]));
  GAP();
}
template <int Gr, int... Js> DI void qgran(Ctx& c, iseq<Js...>) { (qgap<Gr, Js>(c), ...); }

template <int I> DI void store_part_a(Ctx& c) {
  constexpr int t = I >> 1, j = I & 1, dt = t / 3, qt = t % 3;
  asm volatile("" : "+a"(c.o[dt][qt]));
  const f32x16& a = c.o[dt][qt];
  u32x4 v = {cvtpk(a[8 * j], a[8 * j + 1]), cvtpk(a[8 * j + 2], a[8 * j + 3]), cvtpk(a[8 * j + 4], a[8 * j + 5]), cvtpk(a[8 * j + 6], a[8 * j + 7])};
  __builtin_amdgcn_raw_buffer_store_b128(v, c.srdP, c.vpart, I * 1024, PSTORE_AUX);
}
template <int I> DI void store_part_b(Ctx& c) {
  constexpr int t = I >> 1, j = I & 1, dt = t / 3, qt = t % 3;
  const f32x16& a = c.s[dt][qt];
  u32x4 v = {cvtpk(a[8 * j], a[8 * j + 1]), cvtpk(a[8 * j + 2], a[8 * j + 3]), cvtpk(a[8 * j + 4], a[8 * j + 5]), cvtpk(a[8 * j + 6], a[8 * j + 7])};
  __builtin_amdgcn_raw_buffer_store_b128(v, c.srdP, c.vpart, (48 + I) * 1024, PSTORE_AUX);
}
template <int PASS, int Gr, int J> DI void ogap(Ctx& c) {
  constexpr int dt = J / 3, qt = J % 3, IB = 312 + 32 * PASS, GG = 16 * PASS + Gr;
  constexpr int ISS = NRING + IB + 2 * Gr + (J > 1) + (J > 4);
  constexpr int YS = (PASS == 1 && Gr > 4) ? Gr - 4 : 0;
  if constexpr (PASS == 0) mfma_o<Gr == 0>(c.o[dt][qt], c.vf[GG & 1][dt], c.pf[GG & 1][qt]);
  else mfma_v<Gr == 0>(c.s[dt][qt], c.vf[GG & 1][dt], c.pf[GG & 1][qt]);
  if constexpr (GG + 1 < 32) {
    constexpr int G1 = GG + 1, k1 = G1 & 15;
    if constexpr (J == 0) { wait_gran<312 + 2 * G1, 2, ISS, YS>(); rd_v<312 + 2 * G1, 0>(c, G1 & 1); }
    if constexpr (J == 2) { rd_v<312 + 2 * G1, 1>(c, G1 & 1); rd_p<(k1 >> 3), k1 & 7, 2>(c, G1 & 1); }
    if constexpr (J == 3) { rd_p<(k1 >> 3), k1 & 7, 0>(c, G1 & 1); rd_p<(k1 >> 3), k1 & 7, 1>(c, G1 & 1); }
  }
  if constexpr (J == 1 || J == 4) issue_piece<IB + 2 * Gr + NRING + (J == 4)>(c, dep8(c.vf[GG & 1][1]));
  if constexpr (PASS == 1 && Gr >= 4 && J == 5) store_part_a<Gr - 4>(c);
  GAP();
}
template <int PASS, int Gr, int... Js> DI void ogran(Ctx& c, iseq<Js...>) { (ogap<PASS, Gr, Js>(c), ...); }
template <int PASS, int... Gs> DI void opass(Ctx& c, iseq<Gs...>) { (ogran<PASS, Gs>(c, make_iseq<6>{}), ...); }
template <int... Is> DI void store_all_a(Ctx& c, iseq<Is...>) { (store_part_a<Is>(c), ...); }
template <int... Is> DI void store_all_b(Ctx& c, iseq<Is...>) { (store_part_b<Is>(c), ...); }

extern "C" __global__ __launch_bounds__(256, 1)
void attn_kernel(const unsigned short* __restrict__ x, const unsigned short* __restrict__ kc, const unsigned short* __restrict__ vc,
                 const unsigned short* __restrict__ wq, const unsigned short* __restrict__ wo, unsigned short* __restrict__ part) {
  extern __shared__ __attribute__((aligned(16))) char lds[];
  const unsigned ldsb = (unsigned)(uintptr_t)(LDS_AS char*)lds;
  const unsigned tid = threadIdx.x, lane = tid & 63u, l32 = lane & 31u, hi = lane >> 5;
  const unsigned w = __builtin_amdgcn_readfirstlane(tid >> 6);
  const unsigned bid = blockIdx.x, b = bid & 7u, idx = bid >> 3, h = idx >> 1, half = idx & 1u;
  const unsigned row0 = b * SQ + half * 96u;
  Ctx c;
  c.srdK = __builtin_amdgcn_make_buffer_rsrc((void*)(kc + (size_t)(b * NH + h) * SK * DH), (short)0, SK * DH * 2, 0x00020000);
  c.srdV = __builtin_amdgcn_make_buffer_rsrc((void*)(vc + (size_t)(b * NH + h) * SK * DH), (short)0, SK * DH * 2, 0x00020000);
  c.srdW = __builtin_amdgcn_make_buffer_rsrc((void*)wq, (short)0, DM * HD * 2, 0x00020000);
  c.srdX = __builtin_amdgcn_make_buffer_rsrc((void*)x, (short)0, NB * SQ * DM * 2, 0x00020000);
  {
    const unsigned a = lane >> 3, p7 = lane & 7u;
    c.voffK[0] = a * 512u + ((p7 ^ (a >> 1)) * 16u);
    c.voffK[1] = a * 512u + ((p7 ^ (a >> 1) ^ 4u) * 16u);
    c.voffV = ((a & 3u) + 8u * (a >> 2)) * 512u + ((p7 ^ ((a & 2u) << 1)) * 16u);
    c.voffW = a * 8192u + ((p7 ^ ((a & 2u) << 1)) * 16u);
    c.sK = w * 32u * 512u; c.sV = w * 128u; c.sW = h * 512u + w * 128u;
    c.ringM0 = ldsb + LDS_RING + w * (NRING * 1024u);
    c.vK = c.ringM0 + l32 * 128u + ((hi ^ ((l32 >> 1) & 7u)) * 16u);
    const unsigned g16 = lane >> 4, dblk = g16 & 1u, i16 = lane & 15u, q4 = i16 >> 2, p4 = i16 & 3u;
    c.vV = c.ringM0 + (8u * hi + q4) * 128u + (((2u * dblk + (p4 >> 1)) ^ ((q4 >> 1) << 2)) * 16u) + 8u * (p4 & 1u);
    c.vP = ldsb + LDS_P + l32 * 256u + ((hi ^ (l32 & 15u)) * 16u);
    c.vPw = ldsb + LDS_P + l32 * 256u + (((4u * w + hi) ^ (l32 & 15u)) * 16u);
    c.vMx = ldsb + LDS_MX + l32 * 16u; c.vMxw = c.vMx + w * 4u;
  }
  c.srdO = __builtin_amdgcn_make_buffer_rsrc((void*)wo, (short)0, HD * DM * 2, 0x00020000);
  c.voffO = (lane >> 3) * 896u + (((lane & 7u) ^ (((lane >> 3) & 2u) << 1)) * 16u); c.sO = h * 256u * 896u + w * 128u;
  c.srdP = __builtin_amdgcn_make_buffer_rsrc((void*)(part + (size_t)((b * NH + h) * 2u + half) * 84u * 512u), (short)0, 84 * 1024, 0x00020000);
  c.vpart = w * 12u * 1024u + lane * 16u;
  auto load_x = [&](int cc) {
#pragma unroll
    for (int i = 0; i < 6; ++i) {
      const unsigned pc = 6u * w + i;
      const unsigned voff = (lane >> 4) * 896u + ((((lane & 15u) ^ (lane >> 4)) ^ (4u * (pc & 3u))) * 16u);
      dma(ldsb + LDS_P + (unsigned)(cc & 1) * 24576u + pc * 1024u, voff, c.srdX, (row0 + 4u * pc) * 896u + (unsigned)cc * 256u);
    }
  };
  load_x(0);
  [&]<int... Is>(iseq<Is...>) { (issue_piece<Is>(c, 0u), ...); }(make_iseq<NRING>{});
  load_x(1);
  {
    const unsigned scr = ldsb + LDS_SCR + w * 256u;
    const unsigned g = (blockIdx.x * 4u + w) * 16u + (lane & 15u);
    const unsigned woff = (lane < 16u) ? (192u + (g >> 6)) * 8192u + (g & 63u) * 128u : 0xFFFFFF00u;
    asm volatile("s_mov_b32 m0, %0\n\ts_nop 0\n\tbuffer_load_dword %1, %2, 0 offen lds" :: "s"(scr), "v"(woff), "s"(c.srdW) : "memory");
    const unsigned xg = (blockIdx.x * 4u + w) * 5u + lane;
    const unsigned xr = xg / 3u, xc = xg - 3u * xr;
    const unsigned xoff = (lane < 5u && xg < 4608u) ? xr * 896u + 512u + xc * 128u : 0xFFFFFF00u;
    asm volatile("s_mov_b32 m0, %0\n\ts_nop 0\n\tbuffer_load_dword %1, %2, 0 offen lds" :: "s"(scr), "v"(xoff), "s"(c.srdX) : "memory");
  }
  wait_vm<NRING + 6 + 2>();
  barrier();
  wait_gran<0, 2, NRING>(); rd_v<0, 0>(c, 0); rd_v<0, 1>(c, 0);
  rd_p<0, 0, 0>(c, 0); rd_p<0, 0, 1>(c, 0); rd_p<0, 0, 2>(c, 0);
  GAP();
  [&]<int... Gs>(iseq<Gs...>) {
    ([&] {
      qgran<Gs>(c, make_iseq<6>{});
      if constexpr ((Gs & 7) == 7) {
        constexpr int cc1 = (Gs + 1) >> 3;
        wait_vm<14>();
        barrier();
        rd_p<cc1 & 1, 0, 0>(c, (Gs + 1) & 1); rd_p<cc1 & 1, 0, 1>(c, (Gs + 1) & 1); rd_p<cc1 & 1, 0, 2>(c, (Gs + 1) & 1);
        if constexpr (cc1 + 1 < 4) load_x(cc1 + 1);
        GAP();
      }
    }(), ...);
  }(make_iseq<28>{});
  barrier();
  asm volatile("s_nop 7\n\ts_nop 7" : "+a"(c.o[0][0]), "+a"(c.o[0][1]), "+a"(c.o[0][2]), "+a"(c.o[1][0]), "+a"(c.o[1][1]), "+a"(c.o[1][2]));
#pragma unroll
  for (int qt = 0; qt < 3; ++qt)
#pragma unroll
    for (int dt = 0; dt < 2; ++dt)
#pragma unroll
      for (int g = 0; g < 4; ++g) {
        u32x2 v = {cvtpk(c.o[dt][qt][4 * g], c.o[dt][qt][4 * g + 1]), cvtpk(c.o[dt][qt][4 * g + 2], c.o[dt][qt][4 * g + 3])};
        const unsigned ci = 8u * w + 4u * dt + g;
        *(LDS_AS u32x2*)(uintptr_t)(ldsb + LDS_P + (32u * qt + l32) * 512u + ((ci ^ (l32 & 15u)) * 16u) + 8u * hi) = v;
      }
  barrier();
#pragma unroll
  for (int qt = 0; qt < 3; ++qt)
#pragma unroll
    for (int ks = 0; ks < 16; ++ks)
      c.q[qt * 16 + ks] = lds_b128(ldsb + LDS_P + (32u * qt + l32) * 512u + (((2u * ks + hi) ^ (l32 & 15u)) * 16u));
  GAP();
  interval<-1>(c);
  {
#pragma unroll
    for (int qt = 0; qt < 3; ++qt) {
      f32x4 v = *(const LDS_AS f32x4*)(uintptr_t)(c.vMx + (unsigned)(qt * 512));
      c.m[qt] = fmaxf(fmaxf(v[0], v[1]), fmaxf(v[2], v[3])); c.l[qt] = 0.f; c.mc[qt] = -c.m[qt] * SCL;
    }
  }
  GAP();
  interval<0>(c); interval<1>(c); interval<2>(c); interval<3>(c); interval<4>(c);
  interval<5>(c); interval<6>(c); interval<7>(c);
  interval<8>(c);
  asm volatile("s_nop 7\n\ts_nop 7" : "+a"(c.o[0][0]), "+a"(c.o[0][1]), "+a"(c.o[0][2]), "+a"(c.o[1][0]), "+a"(c.o[1][1]), "+a"(c.o[1][2]));
#pragma unroll
  for (int qt = 0; qt < 3; ++qt) {
    const float inv = c.alpha[qt];
#pragma unroll
    for (int dt = 0; dt < 2; ++dt)
#pragma unroll
      for (int g = 0; g < 4; ++g) {
        u32x2 o2 = {cvtpk(c.o[dt][qt][4 * g] * inv, c.o[dt][qt][4 * g + 1] * inv), cvtpk(c.o[dt][qt][4 * g + 2] * inv, c.o[dt][qt][4 * g + 3] * inv)};
        const unsigned ci = 8u * (w & 1u) + 4u * dt + g;
        *(LDS_AS u32x2*)(uintptr_t)(ldsb + LDS_P + (w >> 1) * 24576u + (32u * qt + l32) * 256u + ((ci ^ (l32 & 15u)) * 16u) + 8u * hi) = o2;
      }
  }
  barrier();
  rd_p<0, 0, 0>(c, 0); rd_p<0, 0, 1>(c, 0); rd_p<0, 0, 2>(c, 0);
  GAP();
  opass<0>(c, make_iseq<16>{});
  asm volatile("s_nop 7\n\ts_nop 7" : "+a"(c.o[0][0]), "+a"(c.o[0][1]), "+a"(c.o[0][2]), "+a"(c.o[1][0]), "+a"(c.o[1][1]), "+a"(c.o[1][2]));
  if (w < 3u) {
    opass<1>(c, make_iseq<16>{});
    asm volatile("s_nop 7\n\ts_nop 7" : "+v"(c.s[0][0]), "+v"(c.s[0][1]), "+v"(c.s[0][2]), "+v"(c.s[1][0]), "+v"(c.s[1][1]), "+v"(c.s[1][2]));
    store_all_b(c, make_iseq<12>{});
  } else {
    asm volatile("s_nop 7\n\ts_nop 7" : "+a"(c.o[0][0]), "+a"(c.o[0][1]), "+a"(c.o[0][2]), "+a"(c.o[1][0]), "+a"(c.o[1][1]), "+a"(c.o[1][2]));
    store_all_a(c, make_iseq<12>{});
  }
}

extern "C" __global__ __launch_bounds__(256)
void reduce_kernel(const unsigned short* __restrict__ part, unsigned short* __restrict__ out) {
  const unsigned idx = blockIdx.x * 256u + threadIdx.x, lane = idx & 63u, rest = idx >> 6;
  const unsigned blk = rest % 84u, bh2 = rest / 84u, half = bh2 & 1u, b = bh2 >> 1;
  const u32x4* p = (const u32x4*)(part + ((size_t)((b * NH) * 2u + half) * 84u + blk) * 512u + lane * 8u);
  u32x4 v[16];
#pragma unroll
  for (int h = 0; h < 16; ++h) v[h] = p[(size_t)h * (2u * 84u * 64u)];
  float acc[8] = {0.f, 0.f, 0.f, 0.f, 0.f, 0.f, 0.f, 0.f};
#pragma unroll
  for (int h = 0; h < 16; ++h)
#pragma unroll
    for (int e = 0; e < 4; ++e) { acc[2 * e] += __uint_as_float(v[h][e] << 16); acc[2 * e + 1] += __uint_as_float(v[h][e] & 0xFFFF0000u); }
  const unsigned slice = blk / 12u, tj = blk - 12u * slice, tile = tj >> 1, j = tj & 1u, dt = tile / 3u, qt = tile - 3u * dt;
  const unsigned row = b * SQ + half * 96u + 32u * qt + (lane & 31u), n0 = 64u * slice + 32u * dt + 16u * j + 4u * (lane >> 5);
  u32x2 o0 = {cvtpk(acc[0], acc[1]), cvtpk(acc[2], acc[3])}, o1 = {cvtpk(acc[4], acc[5]), cvtpk(acc[6], acc[7])};
  *(u32x2*)(out + (size_t)row * DM + n0) = o0;
  *(u32x2*)(out + (size_t)row * DM + n0 + 8u) = o1;
}

extern "C" void kernel_launch(void* const* d_in, const int* in_sizes, int n_in, void* d_out, int out_size, void* d_ws, size_t ws_size, hipStream_t stream) {
  const unsigned short* x = (const unsigned short*)d_in[0]; const unsigned short* kc = (const unsigned short*)d_in[1];
  const unsigned short* vc = (const unsigned short*)d_in[2]; const unsigned short* wq = (const unsigned short*)d_in[3];
  const unsigned short* wo = (const unsigned short*)d_in[4];
  char* ws = (char*)d_ws;
  unsigned short* part = (unsigned short*)ws;
  (void)hipFuncSetAttribute((const void*)attn_kernel, hipFuncAttributeMaxDynamicSharedMemorySize, (int)LDS_TOTAL);
  attn_kernel<<<256, 256, LDS_TOTAL, stream>>>(x, kc, vc, wq, wo, part);
  reduce_kernel<<<336, 256, 0, stream>>>(part, (unsigned short*)d_out);
}
```
